```python
import torch

def _softmax(x, axis=-1):
    return torch.softmax(x, dim=axis)
def _gelu(x):
    return torch.nn.functional.gelu(x, approximate="tanh")
def _silu(x):
    return torch.nn.functional.silu(x)
def _sigmoid(x):
    return torch.sigmoid(x)
def _layernorm(x, axis=-1, eps=1e-5):
    m = x.mean(dim=axis, keepdim=True)
    v = x.var(dim=axis, keepdim=True, unbiased=False)
    return (x - m) / torch.sqrt(v + eps)
def _scatter_add(tgt, idx, src, axis=0):
    return tgt.clone().index_add_(axis, idx, src)
def _segment_sum(src, seg, n):
    out = torch.zeros((n,) + src.shape[1:], dtype=src.dtype)
    return out.index_add_(0, seg, src)
def _pad(x, pw):
    flat = tuple(v for pair in reversed(pw) for v in pair)
    return torch.nn.functional.pad(x, flat)

kernel_name = 'gated_mix__ssm_conv_s4__ssm_conv_h3__prompt'

def setup_inputs():

    _w = (lambda a: (lambda t: t.to(torch.bfloat16) if t.is_floating_point() else t)(torch.as_tensor(a)))
    def _A_setup():
        B, L, D, N = 1, 2048, 512, 128
        A_re = -(lambda _x: torch.exp(torch.as_tensor(_x)))((lambda *s: torch.randn(*s, dtype=torch.bfloat16) * 6.4)(D, N))
        A_im = torch.pi * (lambda *s: torch.randn(*s, dtype=torch.bfloat16) * 0.05)(D, N)
        B_re = (lambda *s: torch.randn(*s, dtype=torch.bfloat16) * 0.1)(D, N)
        B_im = (lambda *s: torch.randn(*s, dtype=torch.bfloat16) * 0.1)(D, N)
        C_re = (lambda *s: torch.randn(*s, dtype=torch.bfloat16) * 0.1)(D, N)
        C_im = (lambda *s: torch.randn(*s, dtype=torch.bfloat16) * 0.1)(D, N)
        log_dt = (lambda *s: torch.randn(*s, dtype=torch.bfloat16) * 0.05)(D) * 0.5 - 4.0
        Dskip = (lambda *s: torch.randn(*s, dtype=torch.bfloat16) * 0.05)(D)
        return {
            "u": _w((lambda *s: torch.randn(*s, dtype=torch.bfloat16) * 0.05)(B, L, D)),
            "A_re": _w(A_re),
            "A_im": _w(A_im),
            "B_re": _w(B_re),
            "B_im": _w(B_im),
            "C_re": _w(C_re),
            "C_im": _w(C_im),
            "log_dt": _w(log_dt),
            "Dskip": _w(Dskip),
        }
    _A_in = _A_setup()

    def _B_setup():
        B, S, D = 1, 2048, 512
        ks = 4
        return {
            "q": _w((lambda *s: torch.randn(*s, dtype=torch.bfloat16) * 0.05)(B, S, D)),
            "k": _w((lambda *s: torch.randn(*s, dtype=torch.bfloat16) * 0.4)(B, S, D)),
            "v": _w((lambda *s: torch.randn(*s, dtype=torch.bfloat16) * 0.4)(B, S, D)),
            "shift_k": _w((lambda *s: torch.randn(*s, dtype=torch.bfloat16) * 0.4)(D, ks) * 0.5),
            "ssm_k": _w((lambda *s: torch.randn(*s, dtype=torch.bfloat16) * 0.4)(D, S) * (1.0 / S ** 0.5)),
            "ssm_D": _w((lambda *s: torch.randn(*s, dtype=torch.bfloat16) * 0.4)(D)),
        }
    _B_in = _B_setup()

    return {
        "A_u": _A_in["u"],
        "A_A_re": _A_in["A_re"],
        "A_A_im": _A_in["A_im"],
        "A_B_re": _A_in["B_re"],
        "A_B_im": _A_in["B_im"],
        "A_C_re": _A_in["C_re"],
        "A_C_im": _A_in["C_im"],
        "A_log_dt": _A_in["log_dt"],
        "A_Dskip": _A_in["Dskip"],
        "B_q": _B_in["q"],
        "B_k": _B_in["k"],
        "B_v": _B_in["v"],
        "B_shift_k": _B_in["shift_k"],
        "B_ssm_k": _B_in["ssm_k"],
        "B_ssm_D": _B_in["ssm_D"],
        "gate_w": _w((lambda *s: torch.randn(*s, dtype=torch.bfloat16) * 0.05)(512) * 16.0),
    }

def reference(A_u, A_A_re, A_A_im, A_B_re, A_B_im, A_C_re, A_C_im, A_log_dt, A_Dskip, B_q, B_k, B_v, B_shift_k, B_ssm_k, B_ssm_D, gate_w):
    def _A_ref(u, A_re, A_im, B_re, B_im, C_re, C_im, log_dt, Dskip):
        Bsz, L, D = u.shape
        dt = torch.exp(log_dt)[:, None]
        A = A_re + 1j * A_im
        Bc = B_re + 1j * B_im
        Cc = C_re + 1j * C_im
        logdA = dt * A
        dB = dt * Bc
        l = (lambda x, dt: x.to(dt))(torch.arange(L), A_re.dtype)
        P = torch.exp(logdA[:, :, None] * l[None, None, :])
        K = torch.einsum("dn,dnl,dn->dl", Cc, P, dB).real
        L2 = 2 * L
        Kp = _pad(K, ((0, 0), (0, L)))
        up = (lambda x, *d: x.permute(*d))(u, 0, 2, 1)
        up = _pad(up, ((0, 0), (0, 0), (0, L)))
        Kf = (lambda x, axes=(-2,-1): torch.fft.rfft2(x.float(), dim=axes))(Kp[:, None, :], axes=(-2, -1))
        Uf = (lambda x, axes=(-2,-1): torch.fft.rfft2(x.float(), dim=axes))(up[:, :, None, :], axes=(-2, -1))
        Yf = Uf * Kf[None, :, :, :]
        y = (lambda x, s, axes=(-2,-1): torch.fft.irfft2(x, s=s, dim=axes).to(torch.bfloat16))(Yf, (1, L2), axes=(-2, -1))[:, :, 0, :L]
        y = (lambda x, *d: x.permute(*d))(y, 0, 2, 1) + u * Dskip[None, None, :]
        return y

    def _B_ref(q, k, v, shift_k, ssm_k, ssm_D):
        B, S, D = q.shape
        ks = shift_k.shape[-1]
        k_pad = _pad(k, ((0, 0), (ks - 1, 0), (0, 0)))
        k_shift = torch.zeros_like(k)
        for t in range(ks):
            k_shift = k_shift + k_pad[:, t:t + S, :] * shift_k[:, ks - 1 - t][None, None, :]
        kv = k_shift * v
        L = 2 * S
        kv_pad = _pad(kv, ((0, 0), (0, S), (0, 0)))
        kf_pad = _pad(ssm_k, ((0, 0), (0, S)))
        kv_f = (lambda x, axes=(-2,-1): torch.fft.rfft2(x.float(), dim=axes))(kv_pad[:, None, :, :], axes=(1, 2))
        kf_f = (lambda x, axes=(-2,-1): torch.fft.rfft2(x.float(), dim=axes))(kf_pad[:, None, :], axes=(1, 2))
        kf_f = (lambda x, *d: x.permute(*d))(kf_f, 1, 2, 0)
        y_f = kv_f * kf_f[None, :, :, :]
        y = (lambda x, s, axes=(-2,-1): torch.fft.irfft2(x, s=s, dim=axes).to(torch.bfloat16))(y_f, (1, L), axes=(1, 2))[:, 0, :S, :]
        y = y + kv * ssm_D[None, None, :]
        return q * y

    x = A_u
    g = _sigmoid((x * gate_w).sum(-1, keepdims=True))
    a = _A_ref(x, A_A_re, A_A_im, A_B_re, A_B_im, A_C_re, A_C_im, A_log_dt, A_Dskip)
    b = _B_ref(x, B_k, B_v, B_shift_k, B_ssm_k, B_ssm_D)
    return g * a + (1 - g) * b
```

```cpp
#include <hip/hip_runtime.h>
#include <hip/hip_bf16.h>
#include <stdint.h>
#define LROUND 0
typedef unsigned short u16;
static constexpr int L = 2048, D = 512, N = 128;
__device__ __forceinline__ float bf2f(u16 x) { return __uint_as_float(((unsigned)x) << 16); }
__device__ __forceinline__ u16 f2bf(float f) {
  unsigned u = __float_as_uint(f);
  return (u16)((u + 0x7FFF + ((u >> 16) & 1)) >> 16);
}
__device__ __forceinline__ float rbf(float f) { return bf2f(f2bf(f)); }

__global__ void kgen(const u16* Are, const u16* Aim, const u16* Bre, const u16* Bim, const u16* Cre, const u16* Cim,
                     const u16* logdt, float* K) {
  int l = blockIdx.x * 256 + threadIdx.x;
  int d = blockIdx.y;
  float dt = rbf(expf(bf2f(logdt[d])));
  float lf = LROUND ? rbf((float)l) : (float)l;
  float acc = 0.f;
  for (int n = 0; n < N; ++n) {
    int i = d * N + n;
    float ar = dt * bf2f(Are[i]), ai = dt * bf2f(Aim[i]);
    float br = dt * bf2f(Bre[i]), bi = dt * bf2f(Bim[i]);
    float cr = bf2f(Cre[i]), ci = bf2f(Cim[i]);
    float e = expf(ar * lf);
    float s, c;
    sincosf(ai * lf, &s, &c);
    float pr = e * c, pi = e * s;
    float xr = cr * pr - ci * pi, xi = cr * pi + ci * pr;
    acc += xr * br - xi * bi;
  }
  K[d * L + l] = acc;
}

__global__ void prep(const u16* u, const u16* k, const u16* v, const u16* sk, const u16* gw, u16* kv, float* g) {
  int t = blockIdx.x;
  __shared__ float red[512];
  int d = threadIdx.x;
  float ks = 0.f;
  for (int j = 0; j < 4; ++j) {
    int tt = t + j - 3;
    float kk = tt >= 0 ? bf2f(k[tt * D + d]) : 0.f;
    ks = rbf(ks + rbf(kk * bf2f(sk[d * 4 + 3 - j])));
  }
  kv[t * D + d] = f2bf(ks * bf2f(v[t * D + d]));
  red[d] = rbf(bf2f(u[t * D + d]) * bf2f(gw[d]));
  __syncthreads();
  for (int s = 256; s > 0; s >>= 1) {
    if (d < s) red[d] += red[d + s];
    __syncthreads();
  }
  if (d == 0) {
    float x = rbf(red[0]);
    g[t] = rbf(1.f / (1.f + expf(-x)));
  }
}

__global__ void conv(const u16* u, const u16* kv, const float* K, const u16* ssmk, const u16* Dskip, const u16* ssmD,
                     const float* g, u16* out) {
  int t = blockIdx.x;
  int d = threadIdx.x;
  float ya = 0.f, yb = 0.f;
  for (int l = 0; l <= t; ++l) {
    ya += K[d * L + l] * bf2f(u[(t - l) * D + d]);
    yb += bf2f(ssmk[d * L + l]) * bf2f(kv[(t - l) * D + d]);
  }
  float x = bf2f(u[t * D + d]);
  float a = rbf(rbf(ya) + rbf(x * bf2f(Dskip[d])));
  float y = rbf(rbf(yb) + rbf(bf2f(kv[t * D + d]) * bf2f(ssmD[d])));
  float b = rbf(x * y);
  float gg = g[t];
  out[t * D + d] = f2bf(rbf(gg * a) + rbf(rbf(1.f - gg) * b));
}

extern "C" void kernel_launch(void* const* d_in, const int* in_sizes, int n_in, void* d_out, int out_size, void* d_ws,
                              size_t ws_size, hipStream_t stream) {
  const u16* const* in = (const u16* const*)d_in;
  float* K = (float*)d_ws;
  u16* kv = (u16*)(K + D * L);
  float* g = (float*)(kv + D * L);
  kgen<<<dim3(L / 256, D), 256, 0, stream>>>(in[1], in[2], in[3], in[4], in[5], in[6], in[7], K);
  prep<<<L, 512, 0, stream>>>(in[0], in[10], in[11], in[12], in[15], kv, g);
  conv<<<L, 512, 0, stream>>>(in[0], kv, K, in[13], in[8], in[14], g, (u16*)d_out);
}
```

```cpp
#include <hip/hip_runtime.h>
#include <hip/hip_bf16.h>
#include <stdint.h>

typedef unsigned short u16;
typedef unsigned int u32;
typedef __attribute__((ext_vector_type(8))) short bf16x8;
typedef __attribute__((ext_vector_type(4))) float f32x4;
typedef __attribute__((ext_vector_type(2))) float f32x2;
typedef __attribute__((ext_vector_type(2))) __bf16 bf2_t;
typedef __attribute__((ext_vector_type(4))) u32 u32x4;
typedef __attribute__((ext_vector_type(2))) u32 u32x2;

static constexpr int L = 2048, D = 512, N = 128;

__device__ __forceinline__ float bflo(u32 x) { return __uint_as_float(x << 16); }
__device__ __forceinline__ float bfhi(u32 x) { return __uint_as_float(x & 0xffff0000u); }
__device__ __forceinline__ float bf2f(u16 x) { return __uint_as_float(((u32)x) << 16); }
__device__ __forceinline__ u32 pk2(float lo, float hi) {
  f32x2 v = {lo, hi};
  bf2_t r = __builtin_convertvector(v, bf2_t);
  return __builtin_bit_cast(u32, r);
}
__device__ __forceinline__ float rbf(float f) { return bflo(pk2(f, f)); }

struct cpx { float r, i; };
__device__ __forceinline__ cpx cmul(cpx a, cpx b) {
  cpx o;
  o.r = a.r * b.r - a.i * b.i;
  o.i = a.r * b.i + a.i * b.r;
  return o;
}
__device__ __forceinline__ cpx cexp_l(float ar2, float air, float l) {
  float e = __builtin_amdgcn_exp2f(ar2 * l);
  float p = __builtin_amdgcn_fractf(air * l);
  cpx o;
  o.r = e * __builtin_amdgcn_cosf(p);
  o.i = e * __builtin_amdgcn_sinf(p);
  return o;
}

static constexpr int KG_STRIDE = 512 + 32;

__global__ __launch_bounds__(256) void k1(const u16* __restrict__ u, const u16* __restrict__ Are,
                                          const u16* __restrict__ Aim, const u16* __restrict__ Bre,
                                          const u16* __restrict__ Bim, const u16* __restrict__ Cre,
                                          const u16* __restrict__ Cim, const u16* __restrict__ logdt,
                                          const u16* __restrict__ kk, const u16* __restrict__ vv,
                                          const u16* __restrict__ sk, const u16* __restrict__ gw,
                                          u16* __restrict__ urT, u16* __restrict__ kvrT, u16* __restrict__ Ka,
                                          float* __restrict__ gout) {
  __shared__ __attribute__((aligned(16))) char smem[64 * KG_STRIDE + 1344];
  const int tid = threadIdx.x;
  const int lane = tid & 63;
  const int w = __builtin_amdgcn_readfirstlane(tid >> 6);
  const int b = blockIdx.x;
  if (b < 512) {
    const int d = (b & 7) * 64 + (b >> 3);
    const int pi = d * (N / 2) + lane;
    u32 are = ((const u32*)Are)[pi], aim = ((const u32*)Aim)[pi];
    float dt = rbf(expf(bf2f(logdt[d])));
    const float LOG2E = 1.4426950408889634f, INV2PI = 0.15915494309189535f;
    float ar2[2] = {(dt * bflo(are)) * LOG2E, (dt * bfhi(are)) * LOG2E};
    float air[2] = {(dt * bflo(aim)) * INV2PI, (dt * bfhi(aim)) * INV2PI};
    char* row0 = smem + lane * 8;
    auto put = [&](int e, cpx a, cpx c) {
      u32x2 v = {pk2(a.r, a.i), pk2(c.r, c.i)};
      *(u32x2*)(row0 + e * KG_STRIDE) = v;
    };
    if (w == 0) {
      u32 bre = ((const u32*)Bre)[pi], bim = ((const u32*)Bim)[pi];
      u32 cre = ((const u32*)Cre)[pi], cim = ((const u32*)Cim)[pi];
      cpx x[2], s[2];
      {
        cpx B0 = {dt * bflo(bre), dt * bflo(bim)}, C0 = {bflo(cre), bflo(cim)};
        cpx B1 = {dt * bfhi(bre), dt * bfhi(bim)}, C1 = {bfhi(cre), bfhi(cim)};
        x[0] = cmul(C0, B0);
        x[1] = cmul(C1, B1);
      }
      x[0].i = -x[0].i; x[1].i = -x[1].i;
      s[0] = cexp_l(ar2[0], -air[0], 8.f);
      s[1] = cexp_l(ar2[1], -air[1], 8.f);
      cpx s4[2] = {cexp_l(ar2[0], -air[0], 32.f), cexp_l(ar2[1], -air[1], 32.f)};
#pragma unroll
      for (int jj = 0; jj < 4; ++jj) {
        cpx y[2] = {x[0], x[1]};
#pragma unroll
        for (int j = 0; j < 4; ++j) {
          put(48 + jj * 4 + j, y[0], y[1]);
          if (j < 3) { y[0] = cmul(y[0], s[0]); y[1] = cmul(y[1], s[1]); }
        }
        if (jj < 3) { x[0] = cmul(x[0], s4[0]); x[1] = cmul(x[1], s4[1]); }
      }
    } else if (w == 1) {
      cpx e1[2] = {cexp_l(ar2[0], air[0], 1.f), cexp_l(ar2[1], air[1], 1.f)};
      cpx h[2] = {cexp_l(ar2[0], air[0], 128.f), cexp_l(ar2[1], air[1], 128.f)};
      cpx y[2] = {{1.f, 0.f}, {1.f, 0.f}};
      put(0, y[0], y[1]);
      put(8, h[0], h[1]);
      y[0] = e1[0]; y[1] = e1[1];
#pragma unroll
      for (int j = 1; j < 8; ++j) {
        put(j, y[0], y[1]);
        put(8 + j, cmul(y[0], h[0]), cmul(y[1], h[1]));
        if (j < 7) { y[0] = cmul(y[0], e1[0]); y[1] = cmul(y[1], e1[1]); }
      }
    } else if (w == 2) {
      cpx e2[2] = {cexp_l(ar2[0], air[0], 2.f), cexp_l(ar2[1], air[1], 2.f)};
      cpx h[2] = {cexp_l(ar2[0], air[0], 128.f), cexp_l(ar2[1], air[1], 128.f)};
      cpx y[2] = {cexp_l(ar2[0], air[0], 256.f), cexp_l(ar2[1], air[1], 256.f)};
      cpx e4[2] = {cmul(e2[0], e2[0]), cmul(e2[1], e2[1])};
      cpx e6[2] = {cmul(e4[0], e2[0]), cmul(e4[1], e2[1])};
#pragma unroll
      for (int i = 0; i < 2; ++i) {
        put(16 + 4 * i, y[0], y[1]);
        put(17 + 4 * i, cmul(y[0], e2[0]), cmul(y[1], e2[1]));
        put(18 + 4 * i, cmul(y[0], e4[0]), cmul(y[1], e4[1]));
        put(19 + 4 * i, cmul(y[0], e6[0]), cmul(y[1], e6[1]));
        y[0] = cmul(y[0], h[0]); y[1] = cmul(y[1], h[1]);
      }
#pragma unroll
      for (int i = 0; i < 4; ++i) {
        put(24 + 2 * i, y[0], y[1]);
        put(25 + 2 * i, cmul(y[0], e4[0]), cmul(y[1], e4[1]));
        if (i < 3) { y[0] = cmul(y[0], h[0]); y[1] = cmul(y[1], h[1]); }
      }
    } else {
      cpx h[2] = {cexp_l(ar2[0], air[0], 128.f), cexp_l(ar2[1], air[1], 128.f)};
      cpx y[2] = {cexp_l(ar2[0], air[0], 1024.f), cexp_l(ar2[1], air[1], 1024.f)};
      cpx z[2] = {cexp_l(ar2[0], air[0], 1536.f), cexp_l(ar2[1], air[1], 1536.f)};
#pragma unroll
      for (int i = 0; i < 4; ++i) {
        put(32 + i, y[0], y[1]);
        put(36 + i, z[0], z[1]);
        if (i < 3) { y[0] = cmul(y[0], h[0]); y[1] = cmul(y[1], h[1]); }
        z[0] = cmul(z[0], h[0]); z[1] = cmul(z[1], h[1]);
      }
      put(40, z[0], z[1]);
      cpx zero = {0.f, 0.f};
#pragma unroll
      for (int i = 41; i < 48; ++i) put(i, zero, zero);
    }
    __syncthreads();
    u16* ctab = (u16*)(smem + 64 * KG_STRIDE);
    if (w < 3) {
      const int r = lane & 15, h = lane >> 4;
      const char* pa = smem + (w * 16 + r) * KG_STRIDE + h * 16;
      const char* pb = smem + (48 + r) * KG_STRIDE + h * 16;
      f32x4 acc = {0.f, 0.f, 0.f, 0.f};
#pragma unroll
      for (int ks = 0; ks < 8; ++ks) {
        bf16x8 a = *(const bf16x8*)(pa + ks * 64);
        bf16x8 bb = *(const bf16x8*)(pb + ks * 64);
        acc = __builtin_amdgcn_mfma_f32_16x16x32_bf16(a, bb, acc, 0, 0, 0);
      }
      const int j = r;
#pragma unroll
      for (int q = 0; q < 4; ++q) {
        int i = 4 * h + q;
        int ci;
        if (w == 0) ci = (i & 7) + 128 * (i >> 3) + 8 * j;
        else if (w == 1) ci = (i < 8) ? (256 + 64 * (i >> 2) + (i & 3) + 4 * j) : (384 + 32 * ((i - 8) >> 1) + (i & 1) + 2 * j);
        else ci = 512 + 16 * i + j;
        if (ci < 656) ctab[ci] = (u16)pk2(acc[q], acc[q]);
      }
    }
    __syncthreads();
    {
      const int i = tid;
      u32x4 o;
      if (i < 32) {
        o = *(const u32x4*)(ctab + 8 * i);
      } else if (i < 64) {
        const u16* p = ctab + 256 + 4 * (i - 32);
        u32 e0 = p[0], e1 = p[1], e2 = p[2], e3 = p[3], e4 = p[4];
        o = (u32x4){e0 | (e0 << 16), e1 | (e2 << 16), e2 | (e2 << 16), e3 | (e4 << 16)};
      } else if (i < 128) {
        const u16* p = ctab + 384 + 2 * (i - 64);
        u32 e0 = p[0], e1 = p[1], e2 = p[2];
        o = (u32x4){e0 | (e0 << 16), e0 | (e1 << 16), e1 | (e1 << 16), e2 | (e2 << 16)};
      } else {
        const u16* p = ctab + 512 + (i - 128);
        u32 e0 = p[0], e1 = p[1];
        u32 em = (i & 1) ? e1 : e0;
        o = (u32x4){e0 | (e0 << 16), e0 | (e0 << 16), em | (e1 << 16), e1 | (e1 << 16)};
      }
      *(u32x4*)(Ka + d * L + 8 * i) = o;
    }
  } else {
    const int bb = b - 512;
    const int x = bb & 7, tt = bb >> 3;
    const int cp = lane & 31;
    const int t0 = 64 * tt + 8 * (2 * w + (lane >> 5));
    const int col = x * 32 + cp;
    const u32* u32p = (const u32*)u;
    const u32* k32p = (const u32*)kk;
    const u32* v32p = (const u32*)vv;
    u32 ur[8], vr[8], kr[11];
#pragma unroll
    for (int i = 0; i < 11; ++i) {
      int t = t0 - 3 + i;
      kr[i] = (t >= 0) ? k32p[t * 256 + col] : 0u;
    }
#pragma unroll
    for (int i = 0; i < 8; ++i) vr[i] = v32p[(t0 + i) * 256 + col];
#pragma unroll
    for (int i = 0; i < 8; ++i) ur[i] = u32p[(t0 + i) * 256 + col];
    u32x4 skv = *(const u32x4*)(sk + col * 8);
    const int gr = bb * 8 + 2 * w;
    u32x4 g0 = *(const u32x4*)(u + gr * D + lane * 8);
    u32x4 g1 = *(const u32x4*)(u + (gr + 1) * D + lane * 8);
    u32x4 gwv = *(const u32x4*)(gw + lane * 8);

    {
      u32x4 lo, hi;
#pragma unroll
      for (int j = 0; j < 4; ++j) {
        lo[j] = __builtin_amdgcn_perm(ur[6 - 2 * j], ur[7 - 2 * j], 0x05040100u);
        hi[j] = __builtin_amdgcn_perm(ur[6 - 2 * j], ur[7 - 2 * j], 0x07060302u);
      }
      *(u32x4*)(urT + (2 * col) * L + (2040 - t0)) = lo;
      *(u32x4*)(urT + (2 * col + 1) * L + (2040 - t0)) = hi;
    }
    {
      float s0[4] = {bflo(skv[0]), bfhi(skv[0]), bflo(skv[1]), bfhi(skv[1])};
      float s1[4] = {bflo(skv[2]), bfhi(skv[2]), bflo(skv[3]), bfhi(skv[3])};
      u32 kvr[8];
#pragma unroll
      for (int i = 0; i < 8; ++i) {
        float a0, a1;
        {
          u32 p = pk2(bflo(kr[i]) * s0[3], bfhi(kr[i]) * s1[3]);
          a0 = bflo(p); a1 = bfhi(p);
        }
#pragma unroll
        for (int j = 1; j < 4; ++j) {
          u32 p = pk2(bflo(kr[i + j]) * s0[3 - j], bfhi(kr[i + j]) * s1[3 - j]);
          u32 q = pk2(a0 + bflo(p), a1 + bfhi(p));
          a0 = bflo(q); a1 = bfhi(q);
        }
        kvr[i] = pk2(a0 * bflo(vr[i]), a1 * bfhi(vr[i]));
      }
      u32x4 lo, hi;
#pragma unroll
      for (int j = 0; j < 4; ++j) {
        lo[j] = __builtin_amdgcn_perm(kvr[6 - 2 * j], kvr[7 - 2 * j], 0x05040100u);
        hi[j] = __builtin_amdgcn_perm(kvr[6 - 2 * j], kvr[7 - 2 * j], 0x07060302u);
      }
      *(u32x4*)(kvrT + (2 * col) * L + (2040 - t0)) = lo;
      *(u32x4*)(kvrT + (2 * col + 1) * L + (2040 - t0)) = hi;
    }
    {
      float a0 = 0.f, a1 = 0.f;
#pragma unroll
      for (int j = 0; j < 4; ++j) {
        u32 p = pk2(bflo(g0[j]) * bflo(gwv[j]), bfhi(g0[j]) * bfhi(gwv[j]));
        u32 q = pk2(bflo(g1[j]) * bflo(gwv[j]), bfhi(g1[j]) * bfhi(gwv[j]));
        a0 += bflo(p) + bfhi(p);
        a1 += bflo(q) + bfhi(q);
      }
#pragma unroll
      for (int s = 32; s > 0; s >>= 1) {
        a0 += __shfl_xor(a0, s);
        a1 += __shfl_xor(a1, s);
      }
      if (lane < 2) {
        float xx = rbf(lane ? a1 : a0);
        gout[gr + lane] = rbf(1.f / (1.f + expf(-xx)));
      }
    }
  }
}

static constexpr int CS = 4416;
static constexpr int URB = 4640;
static constexpr int CCB = 4 * CS + URB;
static constexpr int YOFF = 4 * CCB;
static constexpr int K2_LDS = YOFF + 4 * 4096;

__global__ __launch_bounds__(512) void k2(const u16* __restrict__ urT, const u16* __restrict__ kvrT,
                                          const u16* __restrict__ Ka, const u16* __restrict__ ssmk,
                                          const float* __restrict__ g, const u16* __restrict__ Dskip,
                                          const u16* __restrict__ ssmD, u16* __restrict__ out) {
  extern __shared__ __attribute__((aligned(16))) char smem[];
  const int tid = threadIdx.x;
  const int lane = tid & 63;
  const int w = __builtin_amdgcn_readfirstlane(tid >> 6);
  const int b = blockIdx.x;
  const int d0 = (b & 7) * 64 + (b >> 3) * 2;
  {
    const int cc = w >> 1;
    const int i = tid & 127;
    const int ch = d0 + (cc >> 1);
    const u16* ksrc = ((cc & 1) ? ssmk : Ka) + ch * L;
    const u16* usrc = ((cc & 1) ? kvrT : urT) + ch * L;
    char* base = smem + cc * CCB;
    u32x4 kl[2], ul[2];
    u32x2 kn[2];
#pragma unroll
    for (int r = 0; r < 2; ++r) {
      int c = i + 128 * r;
      kl[r] = *(const u32x4*)(ksrc + 8 * c);
      kn[r] = (c < 255) ? *(const u32x2*)(ksrc + 8 * c + 8) : (u32x2){0u, 0u};
      ul[r] = *(const u32x4*)(usrc + 8 * c);
    }
    u32x2 k0 = *(const u32x2*)(ksrc);
    auto putk = [&](int c, u32x4 a, u32x2 n) {
      char* p = base + 64 + 16 * c;
      u32 o0 = __builtin_amdgcn_alignbit(a[1], a[0], 16), o1 = __builtin_amdgcn_alignbit(a[2], a[1], 16),
          o2 = __builtin_amdgcn_alignbit(a[3], a[2], 16), o3 = __builtin_amdgcn_alignbit(n[0], a[3], 16),
          o4 = __builtin_amdgcn_alignbit(n[1], n[0], 16);
      *(u32x4*)(p) = a;
      *(u32x4*)(p + CS) = (u32x4){o0, o1, o2, o3};
      *(u32x4*)(p + 2 * CS) = (u32x4){a[1], a[2], a[3], n[0]};
      *(u32x4*)(p + 3 * CS) = (u32x4){o1, o2, o3, o4};
    };
    u32x4 z4 = {0u, 0u, 0u, 0u};
    if (i < 6) {
      int c = (i < 4) ? (i - 4) : (252 + i);
      putk(c, z4, (i == 3) ? k0 : (u32x2){0u, 0u});
    } else if (i < 40) {
      int c = i - 6;
      int z = (c < 2) ? c : (256 + c);
      *(u32x4*)(base + 4 * CS + 16 * z) = z4;
    }
#pragma unroll
    for (int r = 0; r < 2; ++r) {
      int c = i + 128 * r;
      putk(c, kl[r], kn[r]);
      *(u32x4*)(base + 4 * CS + 32 + 16 * c) = ul[r];
    }
  }
  __syncthreads();
  if (w < 4) {
    const int m = lane & 15, h = lane >> 4;
    const char* base = smem + w * CCB;
    const int e = (m + 1) & 3;
    const char* pa = base + e * CS + 2 * (m + 8 * h + 1 - e);
    int oa2 = w * CCB + e * CS + 2 * (m + 8 * h + 1 - e) + 8;
    asm volatile("" : "+v"(oa2));
    const char* pa2 = smem + oa2;
    const char* pb = base + 4 * CS + 480 - 32 * m + 16 * h;
    f32x4 acc[8];
#pragma unroll
    for (int q = 0; q < 8; ++q) acc[q] = (f32x4){0.f, 0.f, 0.f, 0.f};
    auto ldA = [&](int s) {
      u32x2 lo = *(const u32x2*)(pa + 64 * s);
      u32x2 hi = *(const u32x2*)(pa2 + 64 * s);
      u32x4 v = {lo[0], lo[1], hi[0], hi[1]};
      return __builtin_bit_cast(bf16x8, v);
    };
    auto ldB = [&](int r) { return *(const bf16x8*)(pb + 64 * (56 - r)); };
#pragma unroll
    for (int bq = 0; bq < 8; ++bq) {
      bf16x8 A[8], B[8];
#pragma unroll
      for (int a = 0; a < 8; ++a) { A[a] = ldA(8 * a + bq); B[a] = ldB(8 * a - bq); }
#pragma unroll
      for (int a = 0; a < 8; ++a)
#pragma unroll
        for (int p = 0; p + a < 8; ++p)
          acc[a + p] = __builtin_amdgcn_mfma_f32_16x16x32_bf16(A[a], B[p], acc[a + p], 0, 0, 0);
    }
    {
      bf16x8 Bm = ldB(-8);
#pragma unroll
      for (int q = 0; q < 8; ++q) {
        bf16x8 A = ldA(8 * q + 8);
        acc[q] = __builtin_amdgcn_mfma_f32_16x16x32_bf16(A, Bm, acc[q], 0, 0, 0);
      }
    }
    char* y = smem + YOFF + w * 4096 + 2 * (16 * m + 4 * h);
#pragma unroll
    for (int q = 0; q < 8; ++q) {
      u32x2 v = {pk2(acc[q][0], acc[q][1]), pk2(acc[q][2], acc[q][3])};
      *(u32x2*)(y + 512 * q) = v;
    }
  }
  __syncthreads();
  {
    const int t = tid * 4;
    f32x4 gv = *(const f32x4*)(g + t);
    u32 ds = *(const u32*)(Dskip + d0);
    u32 sd = *(const u32*)(ssmD + d0);
    float res[2][4];
#pragma unroll
    for (int c = 0; c < 2; ++c) {
      u32x2 ya = *(const u32x2*)(smem + YOFF + (2 * c) * 4096 + 2 * t);
      u32x2 yb = *(const u32x2*)(smem + YOFF + (2 * c + 1) * 4096 + 2 * t);
      u32x2 xu = *(const u32x2*)(smem + (2 * c) * CCB + 4 * CS + 2 * (16 + 2044 - t));
      u32x2 xk = *(const u32x2*)(smem + (2 * c + 1) * CCB + 4 * CS + 2 * (16 + 2044 - t));
      float dsk = c ? bfhi(ds) : bflo(ds);
      float sdd = c ? bfhi(sd) : bflo(sd);
      float yav[4] = {bflo(ya[0]), bfhi(ya[0]), bflo(ya[1]), bfhi(ya[1])};
      float ybv[4] = {bflo(yb[0]), bfhi(yb[0]), bflo(yb[1]), bfhi(yb[1])};
      float xv[4] = {bfhi(xu[1]), bflo(xu[1]), bfhi(xu[0]), bflo(xu[0])};
      float kv[4] = {bfhi(xk[1]), bflo(xk[1]), bfhi(xk[0]), bflo(xk[0])};
#pragma unroll
      for (int j = 0; j < 4; ++j) {
        u32 p = pk2(xv[j] * dsk, kv[j] * sdd);
        u32 q = pk2(yav[j] + bflo(p), ybv[j] + bfhi(p));
        float a = bflo(q);
        float bb = rbf(xv[j] * bfhi(q));
        float gg = gv[j];
        u32 r = pk2(gg * a, rbf(1.f - gg) * bb);
        res[c][j] = bflo(r) + bfhi(r);
      }
    }
#pragma unroll
    for (int j = 0; j < 4; ++j) *(u32*)(out + (t + j) * D + d0) = pk2(res[0][j], res[1][j]);
  }
}

extern "C" void kernel_launch(void* const* d_in, const int* in_sizes, int n_in, void* d_out, int out_size, void* d_ws,
                              size_t ws_size, hipStream_t stream) {
  const u16* const* in = (const u16* const*)d_in;
  u16* urT = (u16*)d_ws;
  u16* kvrT = urT + D * L;
  u16* Ka = kvrT + D * L;
  float* g = (float*)(Ka + D * L);
  (void)hipFuncSetAttribute((const void*)k2, hipFuncAttributeMaxDynamicSharedMemorySize, K2_LDS);
  k1<<<768, 256, 0, stream>>>(in[0], in[1], in[2], in[3], in[4], in[5], in[6], in[7], in[10], in[11], in[12], in[15],
                              urT, kvrT, Ka, g);
  k2<<<256, 512, K2_LDS, stream>>>(urT, kvrT, Ka, in[13], g, in[8], in[14], (u16*)d_out);
}
```

```cpp
#include <hip/hip_runtime.h>
#include <hip/hip_bf16.h>
#include <stdint.h>

typedef unsigned short u16;
typedef unsigned int u32;
typedef unsigned long long u64;
typedef __attribute__((ext_vector_type(8))) short bf16x8;
typedef __attribute__((ext_vector_type(4))) float f32x4;
typedef __attribute__((ext_vector_type(2))) float f32x2;
typedef __attribute__((ext_vector_type(2))) __bf16 bf2_t;
typedef __attribute__((ext_vector_type(4))) u32 u32x4;
typedef __attribute__((ext_vector_type(2))) u32 u32x2;
#define GAS __attribute__((address_space(1)))
#define LAS __attribute__((address_space(3)))
typedef GAS u64 gu64;
#define RLX_AGENT __ATOMIC_RELAXED, __HIP_MEMORY_SCOPE_AGENT
#define RLX_WG __ATOMIC_RELAXED, __HIP_MEMORY_SCOPE_WORKGROUP

static constexpr int L = 2048, D = 512, N = 128;

__device__ __forceinline__ float bflo(u32 x) { return __uint_as_float(x << 16); }
__device__ __forceinline__ float bfhi(u32 x) { return __uint_as_float(x & 0xffff0000u); }
__device__ __forceinline__ float bf2f(u16 x) { return __uint_as_float(((u32)x) << 16); }
__device__ __forceinline__ u32 pkv2(f32x2 v) { return __builtin_bit_cast(u32, __builtin_convertvector(v, bf2_t)); }
__device__ __forceinline__ u32 pk2(float lo, float hi) { return pkv2((f32x2){lo, hi}); }
__device__ __forceinline__ float rbf(float f) { return bflo(pk2(f, f)); }
__device__ __forceinline__ float dot2bf(u32 a, u32 b, float c) {
  return __builtin_amdgcn_fdot2_f32_bf16(__builtin_bit_cast(bf2_t, a), __builtin_bit_cast(bf2_t, b), c, false);
}
__device__ __forceinline__ float smul(float a, float b) { float r; asm("v_mul_f32 %0, %1, %2" : "=v"(r) : "v"(a), "v"(b)); return r; }
__device__ __forceinline__ float sfma(float a, float b, float c) { float r; asm("v_fma_f32 %0, %1, %2, %3" : "=v"(r) : "v"(a), "v"(b), "v"(c)); return r; }
__device__ __forceinline__ float sfnma(float a, float b, float c) { float r; asm("v_fma_f32 %0, -%1, %2, %3" : "=v"(r) : "v"(a), "v"(b), "v"(c)); return r; }
__device__ __forceinline__ float sadd(float a, float b) { float r; asm("v_add_f32 %0, %1, %2" : "=v"(r) : "v"(a), "v"(b)); return r; }
__device__ __forceinline__ u32 cvt_once(float a, float b) { u32 r; asm volatile("v_cvt_pk_bf16_f32 %0, %1, %2" : "=v"(r) : "v"(a), "v"(b)); return r; }
__device__ __forceinline__ u32 shf_once(u32 hi, u32 lo, u32 sh) { u32 r; asm volatile("v_alignbit_b32 %0, %1, %2, %3" : "=v"(r) : "v"(hi), "v"(lo), "n"(sh)); return r; }
__device__ __forceinline__ f32x2 cmul(f32x2 a, f32x2 b) {
  const f32x2 n = {-b.y, b.y};
  return __builtin_elementwise_fma(a.yx, n, a * b.xx);
}
__device__ __forceinline__ f32x2 cexp_l(float ar2, float air, float l) {
  const float e = __builtin_amdgcn_exp2f(ar2 * l);
  const float p = __builtin_amdgcn_fractf(air * l);
  return (f32x2){e * __builtin_amdgcn_cosf(p), e * __builtin_amdgcn_sinf(p)};
}

static constexpr int CS = 4416;
static constexpr int URB = 4640;
static constexpr int F_OFF = 0;
static constexpr int KB_OFF = 256;
static constexpr int CTB = 11296;
static constexpr int CT_OFF = KB_OFF;
static constexpr int UR_OFF = KB_OFF + 16 * CS;
static constexpr int KA_OFF = UR_OFF + 4 * URB;
static constexpr int Y_OFF = KA_OFF + 8 * CS;
static constexpr int LDS_BYTES = Y_OFF + 4 * 8192;
static constexpr int ES = 288;
static constexpr int ET = 57 * ES;
static constexpr int WS_GRAN = 4096;
static constexpr int WS_FLAG = 65536;
static constexpr int WS_X = 1 << 20;
static constexpr int XS = 16640;
static constexpr unsigned SPIN_CAP = 1u << 22;

#define CONV_A_ASM \
    "ds_read_b64 v[96:97], %[va] offset:0\n" \
    "ds_read_b64 v[98:99], %[va] offset:8\n" \
    "ds_read_b128 v[100:103], %[vb] offset:3584\n" \
    "ds_read_b128 v[104:107], %[vb] offset:3072\n" \
    "ds_read_b64 v[108:109], %[va] offset:512\n" \
    "ds_read_b64 v[110:111], %[va] offset:520\n" \
    "ds_read_b128 v[112:115], %[vb] offset:2560\n" \
    "s_waitcnt lgkmcnt(4)\n" \
    "v_mfma_f32_16x16x32_bf16 %[c0], v[96:99], v[100:103], 0\n" \
    "ds_read_b64 v[116:117], %[va] offset:1024\n" \
    "ds_read_b64 v[118:119], %[va] offset:1032\n" \
    "s_waitcnt lgkmcnt(2)\n" \
    "v_mfma_f32_16x16x32_bf16 %[c1], v[96:99], v[104:107], 0\n" \
    "ds_read_b128 v[120:123], %[vb] offset:2048\n" \
    "v_mfma_f32_16x16x32_bf16 %[c1], v[108:111], v[100:103], %[c1]\n" \
    "ds_read_b64 v[124:125], %[va] offset:1536\n" \
    "v_mfma_f32_16x16x32_bf16 %[c2], v[108:111], v[104:107], 0\n" \
    "ds_read_b64 v[126:127], %[va] offset:1544\n" \
    "v_mfma_f32_16x16x32_bf16 %[c2], v[96:99], v[112:115], %[c2]\n" \
    "ds_read_b128 v[128:131], %[vb] offset:1536\n" \
    "v_mfma_f32_16x16x32_bf16 %[c3], v[108:111], v[112:115], 0\n" \
    "ds_read_b64 v[132:133], %[va] offset:2048\n" \
    "s_waitcnt lgkmcnt(5)\n" \
    "v_mfma_f32_16x16x32_bf16 %[c2], v[116:119], v[100:103], %[c2]\n" \
    "ds_read_b64 v[134:135], %[va] offset:2056\n" \
    "v_mfma_f32_16x16x32_bf16 %[c3], v[116:119], v[104:107], %[c3]\n" \
    "ds_read_b128 v[136:139], %[vb] offset:1024\n" \
    "v_mfma_f32_16x16x32_bf16 %[c4], v[116:119], v[112:115], 0\n" \
    "ds_read_b64 v[140:141], %[va] offset:2560\n" \
    "s_waitcnt lgkmcnt(6)\n" \
    "v_mfma_f32_16x16x32_bf16 %[c3], v[96:99], v[120:123], %[c3]\n" \
    "ds_read_b64 v[142:143], %[va] offset:2568\n" \
    "v_mfma_f32_16x16x32_bf16 %[c4], v[108:111], v[120:123], %[c4]\n" \
    "ds_read_b128 v[144:147], %[vb] offset:512\n" \
    "v_mfma_f32_16x16x32_bf16 %[c5], v[116:119], v[120:123], 0\n" \
    "ds_read_b64 v[148:149], %[va] offset:3072\n" \
    "s_waitcnt lgkmcnt(6)\n" \
    "v_mfma_f32_16x16x32_bf16 %[c3], v[124:127], v[100:103], %[c3]\n" \
    "ds_read_b64 v[150:151], %[va] offset:3080\n" \
    "v_mfma_f32_16x16x32_bf16 %[c4], v[124:127], v[104:107], %[c4]\n" \
    "ds_read_b128 v[152:155], %[vb] offset:0\n" \
    "v_mfma_f32_16x16x32_bf16 %[c5], v[124:127], v[112:115], %[c5]\n" \
    "ds_read_b64 v[156:157], %[va] offset:3584\n" \
    "v_mfma_f32_16x16x32_bf16 %[c6], v[124:127], v[120:123], 0\n" \
    "ds_read_b64 v[158:159], %[va] offset:3592\n" \
    "v_mfma_f32_16x16x32_bf16 %[c4], v[96:99], v[128:131], %[c4]\n" \
    "ds_read_b64 v[160:161], %[va] offset:256\n" \
    "v_mfma_f32_16x16x32_bf16 %[c5], v[108:111], v[128:131], %[c5]\n" \
    "ds_read_b64 v[162:163], %[va] offset:264\n" \
    "v_mfma_f32_16x16x32_bf16 %[c6], v[116:119], v[128:131], %[c6]\n" \
    "ds_read_b128 v[164:167], %[vb] offset:3840\n" \
    "v_mfma_f32_16x16x32_bf16 %[c7], v[124:127], v[128:131], 0\n" \
    "ds_read_b64 v[168:169], %[va] offset:768\n" \
    "s_waitcnt lgkmcnt(6)\n" \
    "v_mfma_f32_16x16x32_bf16 %[c4], v[132:135], v[100:103], %[c4]\n" \
    "ds_read_b64 v[170:171], %[va] offset:776\n" \
    "v_mfma_f32_16x16x32_bf16 %[c5], v[132:135], v[104:107], %[c5]\n" \
    "ds_read_b64 v[172:173], %[va] offset:1280\n" \
    "v_mfma_f32_16x16x32_bf16 %[c6], v[132:135], v[112:115], %[c6]\n" \
    "ds_read_b64 v[174:175], %[va] offset:1288\n" \
    "v_mfma_f32_16x16x32_bf16 %[c7], v[132:135], v[120:123], %[c7]\n" \
    "ds_read_b64 v[176:177], %[va] offset:1792\n" \
    "v_mfma_f32_16x16x32_bf16 %[c5], v[96:99], v[136:139], %[c5]\n" \
    "ds_read_b64 v[178:179], %[va] offset:1800\n" \
    "v_mfma_f32_16x16x32_bf16 %[c6], v[108:111], v[136:139], %[c6]\n" \
    "ds_read_b64 v[180:181], %[va] offset:2304\n" \
    "v_mfma_f32_16x16x32_bf16 %[c7], v[116:119], v[136:139], %[c7]\n" \
    "ds_read_b64 v[182:183], %[va] offset:2312\n" \
    "v_mfma_f32_16x16x32_bf16 %[c5], v[140:143], v[100:103], %[c5]\n" \
    "ds_read_b64 v[184:185], %[va] offset:2816\n" \
    "v_mfma_f32_16x16x32_bf16 %[c6], v[140:143], v[104:107], %[c6]\n" \
    "ds_read_b64 v[186:187], %[va] offset:2824\n" \
    "v_mfma_f32_16x16x32_bf16 %[c7], v[140:143], v[112:115], %[c7]\n" \
    "ds_read_b64 v[188:189], %[va] offset:3328\n" \
    "v_mfma_f32_16x16x32_bf16 %[c6], v[96:99], v[144:147], %[c6]\n" \
    "ds_read_b64 v[190:191], %[va] offset:3336\n" \
    "v_mfma_f32_16x16x32_bf16 %[c7], v[108:111], v[144:147], %[c7]\n" \
    "ds_read_b64 v[192:193], %[va] offset:3840\n" \
    "v_mfma_f32_16x16x32_bf16 %[c6], v[148:151], v[100:103], %[c6]\n" \
    "ds_read_b64 v[194:195], %[va] offset:3848\n" \
    "v_mfma_f32_16x16x32_bf16 %[c7], v[148:151], v[104:107], %[c7]\n" \
    "ds_read_b64 v[196:197], %[va] offset:64\n" \
    "v_mfma_f32_16x16x32_bf16 %[c7], v[96:99], v[152:155], %[c7]\n" \
    "ds_read_b64 v[198:199], %[va] offset:72\n" \
    "s_waitcnt lgkmcnt(6)\n" \
    "v_mfma_f32_16x16x32_bf16 %[c7], v[156:159], v[100:103], %[c7]\n" \
    "ds_read_b128 v[200:203], %[vb] offset:3648\n" \
    "s_waitcnt lgkmcnt(3)\n" \
    "s_cmp_eq_u32 %[hf], 0\n" \
    "s_cbranch_scc1 LX0_%=\n" \
    "v_mfma_f32_16x16x32_bf16 %[c0], v[160:163], v[164:167], %[c0]\n" \
    "v_mfma_f32_16x16x32_bf16 %[c1], v[168:171], v[164:167], %[c1]\n" \
    "v_mfma_f32_16x16x32_bf16 %[c2], v[172:175], v[164:167], %[c2]\n" \
    "v_mfma_f32_16x16x32_bf16 %[c3], v[176:179], v[164:167], %[c3]\n" \
    "v_mfma_f32_16x16x32_bf16 %[c4], v[180:183], v[164:167], %[c4]\n" \
    "v_mfma_f32_16x16x32_bf16 %[c5], v[184:187], v[164:167], %[c5]\n" \
    "v_mfma_f32_16x16x32_bf16 %[c6], v[188:191], v[164:167], %[c6]\n" \
    "v_mfma_f32_16x16x32_bf16 %[c7], v[192:195], v[164:167], %[c7]\n" \
    "LX0_%=:\n" \
    "ds_read_b128 v[204:207], %[vb] offset:3136\n" \
    "ds_read_b64 v[208:209], %[va] offset:576\n" \
    "s_waitcnt lgkmcnt(2)\n" \
    "v_mfma_f32_16x16x32_bf16 %[c0], v[196:199], v[200:203], %[c0]\n" \
    "ds_read_b64 v[210:211], %[va] offset:584\n" \
    "ds_read_b128 v[212:215], %[vb] offset:2624\n" \
    "s_waitcnt lgkmcnt(3)\n" \
    "v_mfma_f32_16x16x32_bf16 %[c1], v[196:199], v[204:207], %[c1]\n" \
    "ds_read_b64 v[216:217], %[va] offset:1088\n" \
    "ds_read_b64 v[218:219], %[va] offset:1096\n" \
    "s_waitcnt lgkmcnt(3)\n" \
    "v_mfma_f32_16x16x32_bf16 %[c1], v[208:211], v[200:203], %[c1]\n" \
    "ds_read_b128 v[220:223], %[vb] offset:2112\n" \
    "v_mfma_f32_16x16x32_bf16 %[c2], v[208:211], v[204:207], %[c2]\n" \
    "ds_read_b64 v[124:125], %[va] offset:1600\n" \
    "s_waitcnt lgkmcnt(4)\n" \
    "v_mfma_f32_16x16x32_bf16 %[c2], v[196:199], v[212:215], %[c2]\n" \
    "ds_read_b64 v[126:127], %[va] offset:1608\n" \
    "v_mfma_f32_16x16x32_bf16 %[c3], v[208:211], v[212:215], %[c3]\n" \
    "ds_read_b128 v[128:131], %[vb] offset:1600\n" \
    "s_waitcnt lgkmcnt(4)\n" \
    "v_mfma_f32_16x16x32_bf16 %[c2], v[216:219], v[200:203], %[c2]\n" \
    "ds_read_b64 v[132:133], %[va] offset:2112\n" \
    "v_mfma_f32_16x16x32_bf16 %[c3], v[216:219], v[204:207], %[c3]\n" \
    "ds_read_b64 v[134:135], %[va] offset:2120\n" \
    "v_mfma_f32_16x16x32_bf16 %[c4], v[216:219], v[212:215], %[c4]\n" \
    "ds_read_b128 v[120:123], %[vb] offset:1088\n" \
    "s_waitcnt lgkmcnt(6)\n" \
    "v_mfma_f32_16x16x32_bf16 %[c3], v[196:199], v[220:223], %[c3]\n" \
    "ds_read_b64 v[116:117], %[va] offset:2624\n" \
    "v_mfma_f32_16x16x32_bf16 %[c4], v[208:211], v[220:223], %[c4]\n" \
    "ds_read_b64 v[118:119], %[va] offset:2632\n" \
    "v_mfma_f32_16x16x32_bf16 %[c5], v[216:219], v[220:223], %[c5]\n" \
    "ds_read_b128 v[136:139], %[vb] offset:576\n" \
    "s_waitcnt lgkmcnt(6)\n" \
    "v_mfma_f32_16x16x32_bf16 %[c3], v[124:127], v[200:203], %[c3]\n" \
    "ds_read_b64 v[140:141], %[va] offset:3136\n" \
    "v_mfma_f32_16x16x32_bf16 %[c4], v[124:127], v[204:207], %[c4]\n" \
    "ds_read_b64 v[142:143], %[va] offset:3144\n" \
    "v_mfma_f32_16x16x32_bf16 %[c5], v[124:127], v[212:215], %[c5]\n" \
    "ds_read_b128 v[112:115], %[vb] offset:64\n" \
    "v_mfma_f32_16x16x32_bf16 %[c6], v[124:127], v[220:223], %[c6]\n" \
    "ds_read_b64 v[108:109], %[va] offset:3648\n" \
    "v_mfma_f32_16x16x32_bf16 %[c4], v[196:199], v[128:131], %[c4]\n" \
    "ds_read_b64 v[110:111], %[va] offset:3656\n" \
    "v_mfma_f32_16x16x32_bf16 %[c5], v[208:211], v[128:131], %[c5]\n" \
    "ds_read_b64 v[144:145], %[va] offset:128\n" \
    "v_mfma_f32_16x16x32_bf16 %[c6], v[216:219], v[128:131], %[c6]\n" \
    "ds_read_b64 v[146:147], %[va] offset:136\n" \
    "v_mfma_f32_16x16x32_bf16 %[c7], v[124:127], v[128:131], %[c7]\n" \
    "ds_read_b128 v[148:151], %[vb] offset:3712\n" \
    "s_waitcnt lgkmcnt(6)\n" \
    "v_mfma_f32_16x16x32_bf16 %[c4], v[132:135], v[200:203], %[c4]\n" \
    "ds_read_b128 v[104:107], %[vb] offset:3200\n" \
    "v_mfma_f32_16x16x32_bf16 %[c5], v[132:135], v[204:207], %[c5]\n" \
    "ds_read_b64 v[96:97], %[va] offset:640\n" \
    "v_mfma_f32_16x16x32_bf16 %[c6], v[132:135], v[212:215], %[c6]\n" \
    "ds_read_b64 v[98:99], %[va] offset:648\n" \
    "v_mfma_f32_16x16x32_bf16 %[c7], v[132:135], v[220:223], %[c7]\n" \
    "ds_read_b128 v[152:155], %[vb] offset:2688\n" \
    "v_mfma_f32_16x16x32_bf16 %[c5], v[196:199], v[120:123], %[c5]\n" \
    "ds_read_b64 v[156:157], %[va] offset:1152\n" \
    "v_mfma_f32_16x16x32_bf16 %[c6], v[208:211], v[120:123], %[c6]\n" \
    "ds_read_b64 v[158:159], %[va] offset:1160\n" \
    "v_mfma_f32_16x16x32_bf16 %[c7], v[216:219], v[120:123], %[c7]\n" \
    "ds_read_b128 v[100:103], %[vb] offset:2176\n" \
    "v_mfma_f32_16x16x32_bf16 %[c5], v[116:119], v[200:203], %[c5]\n" \
    "ds_read_b64 v[160:161], %[va] offset:1664\n" \
    "v_mfma_f32_16x16x32_bf16 %[c6], v[116:119], v[204:207], %[c6]\n" \
    "ds_read_b64 v[162:163], %[va] offset:1672\n" \
    "v_mfma_f32_16x16x32_bf16 %[c7], v[116:119], v[212:215], %[c7]\n" \
    "v_mfma_f32_16x16x32_bf16 %[c6], v[196:199], v[136:139], %[c6]\n" \
    "ds_read_b128 v[168:171], %[vb] offset:1664\n" \
    "v_mfma_f32_16x16x32_bf16 %[c7], v[208:211], v[136:139], %[c7]\n" \
    "v_mfma_f32_16x16x32_bf16 %[c6], v[140:143], v[200:203], %[c6]\n" \
    "v_mfma_f32_16x16x32_bf16 %[c7], v[140:143], v[204:207], %[c7]\n" \
    "s_waitcnt lgkmcnt(2)\n" \
    "v_mfma_f32_16x16x32_bf16 %[c7], v[196:199], v[112:115], %[c7]\n" \
    "ds_read_b64 v[172:173], %[va] offset:2176\n" \
    "v_mfma_f32_16x16x32_bf16 %[c7], v[108:111], v[200:203], %[c7]\n" \
    "ds_read_b64 v[174:175], %[va] offset:2184\n" \
    "v_mfma_f32_16x16x32_bf16 %[c0], v[144:147], v[148:151], %[c0]\n" \
    "v_mfma_f32_16x16x32_bf16 %[c1], v[144:147], v[104:107], %[c1]\n" \
    "v_mfma_f32_16x16x32_bf16 %[c1], v[96:99], v[148:151], %[c1]\n" \
    "ds_read_b128 v[176:179], %[vb] offset:1152\n" \
    "v_mfma_f32_16x16x32_bf16 %[c2], v[96:99], v[104:107], %[c2]\n" \
    "v_mfma_f32_16x16x32_bf16 %[c2], v[144:147], v[152:155], %[c2]\n" \
    "v_mfma_f32_16x16x32_bf16 %[c3], v[96:99], v[152:155], %[c3]\n" \
    "ds_read_b64 v[180:181], %[va] offset:2688\n" \
    "v_mfma_f32_16x16x32_bf16 %[c2], v[156:159], v[148:151], %[c2]\n" \
    "ds_read_b64 v[182:183], %[va] offset:2696\n" \
    "v_mfma_f32_16x16x32_bf16 %[c3], v[156:159], v[104:107], %[c3]\n" \
    "v_mfma_f32_16x16x32_bf16 %[c4], v[156:159], v[152:155], %[c4]\n" \
    "ds_read_b128 v[184:187], %[vb] offset:640\n" \
    "v_mfma_f32_16x16x32_bf16 %[c3], v[144:147], v[100:103], %[c3]\n" \
    "v_mfma_f32_16x16x32_bf16 %[c4], v[96:99], v[100:103], %[c4]\n" \
    "ds_read_b64 v[188:189], %[va] offset:3200\n" \
    "v_mfma_f32_16x16x32_bf16 %[c5], v[156:159], v[100:103], %[c5]\n" \
    "ds_read_b64 v[190:191], %[va] offset:3208\n" \
    "s_waitcnt lgkmcnt(4)\n" \
    "v_mfma_f32_16x16x32_bf16 %[c3], v[160:163], v[148:151], %[c3]\n" \
    "ds_read_b128 v[192:195], %[vb] offset:128\n" \
    "v_mfma_f32_16x16x32_bf16 %[c4], v[160:163], v[104:107], %[c4]\n" \
    "ds_read_b64 v[164:165], %[va] offset:3712\n" \
    "v_mfma_f32_16x16x32_bf16 %[c5], v[160:163], v[152:155], %[c5]\n" \
    "ds_read_b64 v[166:167], %[va] offset:3720\n" \
    "v_mfma_f32_16x16x32_bf16 %[c6], v[160:163], v[100:103], %[c6]\n" \
    "ds_read_b64 v[124:125], %[va] offset:192\n" \
    "v_mfma_f32_16x16x32_bf16 %[c4], v[144:147], v[168:171], %[c4]\n" \
    "ds_read_b64 v[126:127], %[va] offset:200\n" \
    "v_mfma_f32_16x16x32_bf16 %[c5], v[96:99], v[168:171], %[c5]\n" \
    "ds_read_b128 v[128:131], %[vb] offset:3776\n" \
    "v_mfma_f32_16x16x32_bf16 %[c6], v[156:159], v[168:171], %[c6]\n" \
    "ds_read_b128 v[132:135], %[vb] offset:3264\n" \
    "v_mfma_f32_16x16x32_bf16 %[c7], v[160:163], v[168:171], %[c7]\n" \
    "ds_read_b64 v[220:221], %[va] offset:704\n" \
    "v_mfma_f32_16x16x32_bf16 %[c4], v[172:175], v[148:151], %[c4]\n" \
    "ds_read_b64 v[222:223], %[va] offset:712\n" \
    "v_mfma_f32_16x16x32_bf16 %[c5], v[172:175], v[104:107], %[c5]\n" \
    "ds_read_b128 v[216:219], %[vb] offset:2752\n" \
    "v_mfma_f32_16x16x32_bf16 %[c6], v[172:175], v[152:155], %[c6]\n" \
    "ds_read_b64 v[120:121], %[va] offset:1216\n" \
    "v_mfma_f32_16x16x32_bf16 %[c7], v[172:175], v[100:103], %[c7]\n" \
    "ds_read_b64 v[122:123], %[va] offset:1224\n" \
    "v_mfma_f32_16x16x32_bf16 %[c5], v[144:147], v[176:179], %[c5]\n" \
    "ds_read_b128 v[116:119], %[vb] offset:2240\n" \
    "v_mfma_f32_16x16x32_bf16 %[c6], v[96:99], v[176:179], %[c6]\n" \
    "ds_read_b64 v[212:213], %[va] offset:1728\n" \
    "v_mfma_f32_16x16x32_bf16 %[c7], v[156:159], v[176:179], %[c7]\n" \
    "ds_read_b64 v[214:215], %[va] offset:1736\n" \
    "s_waitcnt lgkmcnt(6)\n" \
    "v_mfma_f32_16x16x32_bf16 %[c5], v[180:183], v[148:151], %[c5]\n" \
    "ds_read_b128 v[208:211], %[vb] offset:1728\n" \
    "v_mfma_f32_16x16x32_bf16 %[c6], v[180:183], v[104:107], %[c6]\n" \
    "ds_read_b64 v[136:137], %[va] offset:2240\n" \
    "v_mfma_f32_16x16x32_bf16 %[c7], v[180:183], v[152:155], %[c7]\n" \
    "ds_read_b64 v[138:139], %[va] offset:2248\n" \
    "v_mfma_f32_16x16x32_bf16 %[c6], v[144:147], v[184:187], %[c6]\n" \
    "ds_read_b128 v[140:143], %[vb] offset:1216\n" \
    "v_mfma_f32_16x16x32_bf16 %[c7], v[96:99], v[184:187], %[c7]\n" \
    "v_mfma_f32_16x16x32_bf16 %[c6], v[188:191], v[148:151], %[c6]\n" \
    "v_mfma_f32_16x16x32_bf16 %[c7], v[188:191], v[104:107], %[c7]\n" \
    "v_mfma_f32_16x16x32_bf16 %[c7], v[144:147], v[192:195], %[c7]\n" \
    "ds_read_b64 v[204:205], %[va] offset:2752\n" \
    "v_mfma_f32_16x16x32_bf16 %[c7], v[164:167], v[148:151], %[c7]\n" \
    "ds_read_b64 v[206:207], %[va] offset:2760\n" \
    "v_mfma_f32_16x16x32_bf16 %[c0], v[124:127], v[128:131], %[c0]\n" \
    "ds_read_b128 v[196:199], %[vb] offset:704\n" \
    "v_mfma_f32_16x16x32_bf16 %[c1], v[124:127], v[132:135], %[c1]\n" \
    "v_mfma_f32_16x16x32_bf16 %[c1], v[220:223], v[128:131], %[c1]\n" \
    "s_waitcnt lgkmcnt(3)\n" \
    "v_mfma_f32_16x16x32_bf16 %[c2], v[124:127], v[216:219], %[c2]\n" \
    "v_mfma_f32_16x16x32_bf16 %[c2], v[220:223], v[132:135], %[c2]\n" \
    "v_mfma_f32_16x16x32_bf16 %[c2], v[120:123], v[128:131], %[c2]\n" \
    "ds_read_b64 v[112:113], %[va] offset:3264\n" \
    "v_mfma_f32_16x16x32_bf16 %[c3], v[124:127], v[116:119], %[c3]\n" \
    "ds_write_b128 %[vy], %[c0] offset:0\n" \
    "v_mfma_f32_16x16x32_bf16 %[c3], v[220:223], v[216:219], %[c3]\n" \
    "ds_read_b64 v[114:115], %[va] offset:3272\n" \
    "v_mfma_f32_16x16x32_bf16 %[c3], v[120:123], v[132:135], %[c3]\n" \
    "ds_write_b128 %[vy], %[c1] offset:1024\n" \
    "v_mfma_f32_16x16x32_bf16 %[c3], v[212:215], v[128:131], %[c3]\n" \
    "ds_read_b128 v[108:111], %[vb] offset:192\n" \
    "v_mfma_f32_16x16x32_bf16 %[c4], v[124:127], v[208:211], %[c4]\n" \
    "ds_write_b128 %[vy], %[c2] offset:2048\n" \
    "v_mfma_f32_16x16x32_bf16 %[c4], v[220:223], v[116:119], %[c4]\n" \
    "v_mfma_f32_16x16x32_bf16 %[c4], v[120:123], v[216:219], %[c4]\n" \
    "v_mfma_f32_16x16x32_bf16 %[c4], v[212:215], v[132:135], %[c4]\n" \
    "ds_read_b64 v[200:201], %[va] offset:3776\n" \
    "v_mfma_f32_16x16x32_bf16 %[c4], v[136:139], v[128:131], %[c4]\n" \
    "ds_read_b64 v[202:203], %[va] offset:3784\n" \
    "v_mfma_f32_16x16x32_bf16 %[c5], v[124:127], v[140:143], %[c5]\n" \
    "ds_write_b128 %[vy], %[c3] offset:3072\n" \
    "v_mfma_f32_16x16x32_bf16 %[c5], v[220:223], v[208:211], %[c5]\n" \
    "v_mfma_f32_16x16x32_bf16 %[c5], v[120:123], v[116:119], %[c5]\n" \
    "v_mfma_f32_16x16x32_bf16 %[c5], v[212:215], v[216:219], %[c5]\n" \
    "v_mfma_f32_16x16x32_bf16 %[c5], v[136:139], v[132:135], %[c5]\n" \
    "s_waitcnt lgkmcnt(2)\n" \
    "v_mfma_f32_16x16x32_bf16 %[c5], v[204:207], v[128:131], %[c5]\n" \
    "ds_write_b128 %[vy], %[c4] offset:4096\n" \
    "v_mfma_f32_16x16x32_bf16 %[c6], v[124:127], v[196:199], %[c6]\n" \
    "v_mfma_f32_16x16x32_bf16 %[c6], v[220:223], v[140:143], %[c6]\n" \
    "v_mfma_f32_16x16x32_bf16 %[c6], v[120:123], v[208:211], %[c6]\n" \
    "v_mfma_f32_16x16x32_bf16 %[c6], v[212:215], v[116:119], %[c6]\n" \
    "v_mfma_f32_16x16x32_bf16 %[c6], v[136:139], v[216:219], %[c6]\n" \
    "v_mfma_f32_16x16x32_bf16 %[c6], v[204:207], v[132:135], %[c6]\n" \
    "v_mfma_f32_16x16x32_bf16 %[c6], v[112:115], v[128:131], %[c6]\n" \
    "v_mfma_f32_16x16x32_bf16 %[c7], v[124:127], v[108:111], %[c7]\n" \
    "ds_write_b128 %[vy], %[c5] offset:5120\n" \
    "v_mfma_f32_16x16x32_bf16 %[c7], v[220:223], v[196:199], %[c7]\n" \
    "v_mfma_f32_16x16x32_bf16 %[c7], v[120:123], v[140:143], %[c7]\n" \
    "v_mfma_f32_16x16x32_bf16 %[c7], v[212:215], v[208:211], %[c7]\n" \
    "v_mfma_f32_16x16x32_bf16 %[c7], v[136:139], v[116:119], %[c7]\n" \
    "v_mfma_f32_16x16x32_bf16 %[c7], v[204:207], v[216:219], %[c7]\n" \
    "v_mfma_f32_16x16x32_bf16 %[c7], v[112:115], v[132:135], %[c7]\n" \
    "s_waitcnt lgkmcnt(0)\n" \
    "v_mfma_f32_16x16x32_bf16 %[c7], v[200:203], v[128:131], %[c7]\n" \
    "ds_write_b128 %[vy], %[c6] offset:6144\n" \
    "s_nop 6\n" \
    "ds_write_b128 %[vy], %[c7] offset:7168\n" \
    "s_waitcnt lgkmcnt(0)\n"
#define CONV_B_ASM \
    "ds_read_b128 v[96:99], %[va] offset:0\n" \
    "ds_read_b128 v[100:103], %[vb] offset:3584\n" \
    "ds_read_b128 v[104:107], %[vb] offset:3072\n" \
    "ds_read_b128 v[108:111], %[va] offset:512\n" \
    "ds_read_b128 v[112:115], %[va] offset:256\n" \
    "ds_read_b128 v[116:119], %[vb] offset:3840\n" \
    "ds_read_b128 v[120:123], %[va] offset:768\n" \
    "ds_read_b128 v[124:127], %[va] offset:64\n" \
    "ds_read_b128 v[128:131], %[vb] offset:3648\n" \
    "s_waitcnt lgkmcnt(7)\n" \
    "v_mfma_f32_16x16x32_bf16 %[c0], v[96:99], v[100:103], 0\n" \
    "ds_read_b128 v[132:135], %[vb] offset:3136\n" \
    "s_waitcnt lgkmcnt(1)\n" \
    "v_mfma_f32_16x16x32_bf16 %[c1], v[96:99], v[104:107], 0\n" \
    "ds_read_b128 v[136:139], %[va] offset:576\n" \
    "v_mfma_f32_16x16x32_bf16 %[c1], v[108:111], v[100:103], %[c1]\n" \
    "ds_read_b128 v[140:143], %[va] offset:128\n" \
    "ds_read_b128 v[144:147], %[vb] offset:3712\n" \
    "s_cmp_eq_u32 %[hf], 0\n" \
    "s_cbranch_scc1 LX0_%=\n" \
    "v_mfma_f32_16x16x32_bf16 %[c0], v[112:115], v[116:119], %[c0]\n" \
    "v_mfma_f32_16x16x32_bf16 %[c1], v[120:123], v[116:119], %[c1]\n" \
    "LX0_%=:\n" \
    "ds_read_b128 v[148:151], %[vb] offset:3200\n" \
    "ds_read_b128 v[152:155], %[va] offset:640\n" \
    "v_mfma_f32_16x16x32_bf16 %[c0], v[124:127], v[128:131], %[c0]\n" \
    "ds_read_b128 v[156:159], %[va] offset:192\n" \
    "ds_read_b128 v[160:163], %[vb] offset:3776\n" \
    "s_waitcnt lgkmcnt(7)\n" \
    "v_mfma_f32_16x16x32_bf16 %[c1], v[124:127], v[132:135], %[c1]\n" \
    "ds_read_b128 v[164:167], %[vb] offset:3264\n" \
    "s_waitcnt lgkmcnt(7)\n" \
    "v_mfma_f32_16x16x32_bf16 %[c1], v[136:139], v[128:131], %[c1]\n" \
    "ds_read_b128 v[168:171], %[va] offset:704\n" \
    "s_waitcnt lgkmcnt(6)\n" \
    "v_mfma_f32_16x16x32_bf16 %[c0], v[140:143], v[144:147], %[c0]\n" \
    "ds_read_b128 v[172:175], %[va] offset:0\n" \
    "ds_read_b128 v[176:179], %[vb] offset:2560\n" \
    "s_waitcnt lgkmcnt(7)\n" \
    "v_mfma_f32_16x16x32_bf16 %[c1], v[140:143], v[148:151], %[c1]\n" \
    "ds_read_b128 v[180:183], %[vb] offset:2048\n" \
    "s_waitcnt lgkmcnt(7)\n" \
    "v_mfma_f32_16x16x32_bf16 %[c1], v[152:155], v[144:147], %[c1]\n" \
    "ds_read_b128 v[184:187], %[vb] offset:1536\n" \
    "s_waitcnt lgkmcnt(6)\n" \
    "v_mfma_f32_16x16x32_bf16 %[c0], v[156:159], v[160:163], %[c0]\n" \
    "ds_read_b128 v[188:191], %[va] offset:512\n" \
    "ds_read_b128 v[192:195], %[vb] offset:3072\n" \
    "s_waitcnt lgkmcnt(7)\n" \
    "v_mfma_f32_16x16x32_bf16 %[c1], v[156:159], v[164:167], %[c1]\n" \
    "ds_read_b128 v[196:199], %[va] offset:1024\n" \
    "s_waitcnt lgkmcnt(7)\n" \
    "v_mfma_f32_16x16x32_bf16 %[c1], v[168:171], v[160:163], %[c1]\n" \
    "ds_read_b128 v[200:203], %[vb] offset:3584\n" \
    "s_waitcnt lgkmcnt(6)\n" \
    "v_mfma_f32_16x16x32_bf16 %[c2], v[172:175], v[176:179], 0\n" \
    "ds_write_b128 %[vy], %[c0] offset:0\n" \
    "s_waitcnt lgkmcnt(6)\n" \
    "v_mfma_f32_16x16x32_bf16 %[c3], v[172:175], v[180:183], 0\n" \
    "ds_read_b128 v[204:207], %[va] offset:1536\n" \
    "s_waitcnt lgkmcnt(6)\n" \
    "v_mfma_f32_16x16x32_bf16 %[c4], v[172:175], v[184:187], 0\n" \
    "ds_write_b128 %[vy], %[c1] offset:1024\n" \
    "s_waitcnt lgkmcnt(5)\n" \
    "v_mfma_f32_16x16x32_bf16 %[c2], v[188:191], v[192:195], %[c2]\n" \
    "ds_read_b128 v[208:211], %[va] offset:2048\n" \
    "v_mfma_f32_16x16x32_bf16 %[c3], v[188:191], v[176:179], %[c3]\n" \
    "ds_read_b128 v[212:215], %[va] offset:1280\n" \
    "v_mfma_f32_16x16x32_bf16 %[c4], v[188:191], v[180:183], %[c4]\n" \
    "ds_read_b128 v[216:219], %[vb] offset:3840\n" \
    "s_waitcnt lgkmcnt(6)\n" \
    "v_mfma_f32_16x16x32_bf16 %[c2], v[196:199], v[200:203], %[c2]\n" \
    "s_waitcnt lgkmcnt(3)\n" \
    "s_barrier\n" \
    "v_mfma_f32_16x16x32_bf16 %[c3], v[196:199], v[192:195], %[c3]\n" \
    "ds_read_b128 v[220:223], %[va] offset:1792\n" \
    "v_mfma_f32_16x16x32_bf16 %[c4], v[196:199], v[176:179], %[c4]\n" \
    "ds_read_b128 v[96:99], %[va] offset:2304\n" \
    "v_mfma_f32_16x16x32_bf16 %[c3], v[204:207], v[200:203], %[c3]\n" \
    "ds_read_b128 v[104:107], %[va] offset:64\n" \
    "ds_read_b128 v[108:111], %[vb] offset:2624\n" \
    "v_mfma_f32_16x16x32_bf16 %[c4], v[204:207], v[192:195], %[c4]\n" \
    "ds_read_b128 v[100:103], %[vb] offset:2112\n" \
    "s_waitcnt lgkmcnt(6)\n" \
    "v_mfma_f32_16x16x32_bf16 %[c4], v[208:211], v[200:203], %[c4]\n" \
    "ds_read_b128 v[112:115], %[vb] offset:1600\n" \
    "s_waitcnt lgkmcnt(4)\n" \
    "s_cmp_eq_u32 %[hf], 0\n" \
    "s_cbranch_scc1 LX1_%=\n" \
    "v_mfma_f32_16x16x32_bf16 %[c2], v[212:215], v[216:219], %[c2]\n" \
    "v_mfma_f32_16x16x32_bf16 %[c3], v[220:223], v[216:219], %[c3]\n" \
    "v_mfma_f32_16x16x32_bf16 %[c4], v[96:99], v[216:219], %[c4]\n" \
    "LX1_%=:\n" \
    "ds_read_b128 v[120:123], %[va] offset:576\n" \
    "ds_read_b128 v[116:119], %[vb] offset:3136\n" \
    "s_waitcnt lgkmcnt(4)\n" \
    "v_mfma_f32_16x16x32_bf16 %[c2], v[104:107], v[108:111], %[c2]\n" \
    "ds_read_b128 v[124:127], %[va] offset:1088\n" \
    "ds_read_b128 v[132:135], %[vb] offset:3648\n" \
    "s_waitcnt lgkmcnt(5)\n" \
    "v_mfma_f32_16x16x32_bf16 %[c3], v[104:107], v[100:103], %[c3]\n" \
    "ds_read_b128 v[136:139], %[va] offset:1600\n" \
    "s_waitcnt lgkmcnt(5)\n" \
    "v_mfma_f32_16x16x32_bf16 %[c4], v[104:107], v[112:115], %[c4]\n" \
    "ds_read_b128 v[128:131], %[va] offset:2112\n" \
    "s_waitcnt lgkmcnt(4)\n" \
    "v_mfma_f32_16x16x32_bf16 %[c2], v[120:123], v[116:119], %[c2]\n" \
    "ds_read_b128 v[140:143], %[va] offset:128\n" \
    "v_mfma_f32_16x16x32_bf16 %[c3], v[120:123], v[108:111], %[c3]\n" \
    "ds_read_b128 v[148:151], %[vb] offset:2688\n" \
    "v_mfma_f32_16x16x32_bf16 %[c4], v[120:123], v[100:103], %[c4]\n" \
    "ds_read_b128 v[152:155], %[vb] offset:2176\n" \
    "s_waitcnt lgkmcnt(5)\n" \
    "v_mfma_f32_16x16x32_bf16 %[c2], v[124:127], v[132:135], %[c2]\n" \
    "ds_read_b128 v[144:147], %[vb] offset:1664\n" \
    "v_mfma_f32_16x16x32_bf16 %[c3], v[124:127], v[116:119], %[c3]\n" \
    "ds_read_b128 v[156:159], %[va] offset:640\n" \
    "v_mfma_f32_16x16x32_bf16 %[c4], v[124:127], v[108:111], %[c4]\n" \
    "ds_read_b128 v[164:167], %[vb] offset:3200\n" \
    "s_waitcnt lgkmcnt(6)\n" \
    "v_mfma_f32_16x16x32_bf16 %[c3], v[136:139], v[132:135], %[c3]\n" \
    "ds_read_b128 v[168:171], %[va] offset:1152\n" \
    "v_mfma_f32_16x16x32_bf16 %[c4], v[136:139], v[116:119], %[c4]\n" \
    "ds_read_b128 v[160:163], %[vb] offset:3712\n" \
    "v_mfma_f32_16x16x32_bf16 %[c4], v[128:131], v[132:135], %[c4]\n" \
    "ds_read_b128 v[172:175], %[va] offset:1664\n" \
    "s_waitcnt lgkmcnt(6)\n" \
    "v_mfma_f32_16x16x32_bf16 %[c2], v[140:143], v[148:151], %[c2]\n" \
    "ds_read_b128 v[184:187], %[va] offset:2176\n" \
    "v_mfma_f32_16x16x32_bf16 %[c3], v[140:143], v[152:155], %[c3]\n" \
    "ds_read_b128 v[188:191], %[va] offset:192\n" \
    "s_waitcnt lgkmcnt(6)\n" \
    "v_mfma_f32_16x16x32_bf16 %[c4], v[140:143], v[144:147], %[c4]\n" \
    "ds_read_b128 v[180:183], %[vb] offset:2752\n" \
    "s_waitcnt lgkmcnt(6)\n" \
    "v_mfma_f32_16x16x32_bf16 %[c2], v[156:159], v[164:167], %[c2]\n" \
    "ds_read_b128 v[196:199], %[va] offset:704\n" \
    "v_mfma_f32_16x16x32_bf16 %[c3], v[156:159], v[148:151], %[c3]\n" \
    "ds_read_b128 v[176:179], %[vb] offset:3264\n" \
    "v_mfma_f32_16x16x32_bf16 %[c4], v[156:159], v[152:155], %[c4]\n" \
    "ds_read_b128 v[204:207], %[va] offset:1216\n" \
    "s_waitcnt lgkmcnt(6)\n" \
    "v_mfma_f32_16x16x32_bf16 %[c2], v[168:171], v[160:163], %[c2]\n" \
    "ds_read_b128 v[192:195], %[vb] offset:3776\n" \
    "v_mfma_f32_16x16x32_bf16 %[c3], v[168:171], v[164:167], %[c3]\n" \
    "ds_read_b128 v[208:211], %[vb] offset:2240\n" \
    "v_mfma_f32_16x16x32_bf16 %[c4], v[168:171], v[148:151], %[c4]\n" \
    "ds_read_b128 v[200:203], %[va] offset:1728\n" \
    "v_mfma_f32_16x16x32_bf16 %[c3], v[172:175], v[160:163], %[c3]\n" \
    "ds_read_b128 v[212:215], %[vb] offset:1728\n" \
    "v_mfma_f32_16x16x32_bf16 %[c4], v[172:175], v[164:167], %[c4]\n" \
    "ds_read_b128 v[220:223], %[va] offset:2240\n" \
    "s_waitcnt lgkmcnt(6)\n" \
    "v_mfma_f32_16x16x32_bf16 %[c4], v[184:187], v[160:163], %[c4]\n" \
    "ds_read_b128 v[96:99], %[va] offset:0\n" \
    "v_mfma_f32_16x16x32_bf16 %[c2], v[188:191], v[180:183], %[c2]\n" \
    "ds_read_b128 v[216:219], %[vb] offset:1024\n" \
    "v_mfma_f32_16x16x32_bf16 %[c2], v[196:199], v[176:179], %[c2]\n" \
    "ds_read_b128 v[104:107], %[vb] offset:512\n" \
    "s_waitcnt lgkmcnt(6)\n" \
    "v_mfma_f32_16x16x32_bf16 %[c2], v[204:207], v[192:195], %[c2]\n" \
    "ds_read_b128 v[112:115], %[vb] offset:0\n" \
    "v_mfma_f32_16x16x32_bf16 %[c3], v[188:191], v[208:211], %[c3]\n" \
    "ds_read_b128 v[120:123], %[va] offset:512\n" \
    "v_mfma_f32_16x16x32_bf16 %[c3], v[196:199], v[180:183], %[c3]\n" \
    "ds_read_b128 v[100:103], %[vb] offset:1536\n" \
    "v_mfma_f32_16x16x32_bf16 %[c3], v[204:207], v[176:179], %[c3]\n" \
    "ds_read_b128 v[124:127], %[va] offset:1024\n" \
    "s_waitcnt lgkmcnt(6)\n" \
    "v_mfma_f32_16x16x32_bf16 %[c3], v[200:203], v[192:195], %[c3]\n" \
    "ds_write_b128 %[vy], %[c2] offset:2048\n" \
    "v_mfma_f32_16x16x32_bf16 %[c4], v[188:191], v[212:215], %[c4]\n" \
    "ds_read_b128 v[108:111], %[vb] offset:2048\n" \
    "v_mfma_f32_16x16x32_bf16 %[c4], v[196:199], v[208:211], %[c4]\n" \
    "ds_read_b128 v[136:139], %[va] offset:1536\n" \
    "v_mfma_f32_16x16x32_bf16 %[c4], v[204:207], v[180:183], %[c4]\n" \
    "ds_read_b128 v[116:119], %[vb] offset:2560\n" \
    "v_mfma_f32_16x16x32_bf16 %[c4], v[200:203], v[176:179], %[c4]\n" \
    "ds_read_b128 v[128:131], %[va] offset:2048\n" \
    "v_mfma_f32_16x16x32_bf16 %[c4], v[220:223], v[192:195], %[c4]\n" \
    "ds_write_b128 %[vy], %[c3] offset:3072\n" \
    "s_waitcnt lgkmcnt(6)\n" \
    "v_mfma_f32_16x16x32_bf16 %[c5], v[96:99], v[216:219], 0\n" \
    "ds_read_b128 v[132:135], %[vb] offset:3072\n" \
    "v_mfma_f32_16x16x32_bf16 %[c6], v[96:99], v[104:107], 0\n" \
    "ds_read_b128 v[140:143], %[va] offset:2560\n" \
    "v_mfma_f32_16x16x32_bf16 %[c7], v[96:99], v[112:115], 0\n" \
    "ds_read_b128 v[144:147], %[vb] offset:3584\n" \
    "v_mfma_f32_16x16x32_bf16 %[c5], v[120:123], v[100:103], %[c5]\n" \
    "ds_write_b128 %[vy], %[c4] offset:4096\n" \
    "v_mfma_f32_16x16x32_bf16 %[c6], v[120:123], v[216:219], %[c6]\n" \
    "ds_read_b128 v[156:159], %[va] offset:3072\n" \
    "v_mfma_f32_16x16x32_bf16 %[c7], v[120:123], v[104:107], %[c7]\n" \
    "ds_read_b128 v[152:155], %[va] offset:3584\n" \
    "s_waitcnt lgkmcnt(6)\n" \
    "v_mfma_f32_16x16x32_bf16 %[c5], v[124:127], v[108:111], %[c5]\n" \
    "ds_read_b128 v[168:171], %[va] offset:2816\n" \
    "v_mfma_f32_16x16x32_bf16 %[c6], v[124:127], v[100:103], %[c6]\n" \
    "s_waitcnt lgkmcnt(3)\n" \
    "s_barrier\n" \
    "v_mfma_f32_16x16x32_bf16 %[c7], v[124:127], v[216:219], %[c7]\n" \
    "ds_read_b128 v[148:151], %[vb] offset:3840\n" \
    "v_mfma_f32_16x16x32_bf16 %[c5], v[136:139], v[116:119], %[c5]\n" \
    "ds_read_b128 v[172:175], %[va] offset:3328\n" \
    "v_mfma_f32_16x16x32_bf16 %[c6], v[136:139], v[108:111], %[c6]\n" \
    "ds_read_b128 v[164:167], %[va] offset:3840\n" \
    "v_mfma_f32_16x16x32_bf16 %[c7], v[136:139], v[100:103], %[c7]\n" \
    "ds_read_b128 v[184:187], %[va] offset:64\n" \
    "v_mfma_f32_16x16x32_bf16 %[c5], v[128:131], v[132:135], %[c5]\n" \
    "ds_read_b128 v[160:163], %[vb] offset:1088\n" \
    "v_mfma_f32_16x16x32_bf16 %[c6], v[128:131], v[116:119], %[c6]\n" \
    "ds_read_b128 v[188:191], %[vb] offset:576\n" \
    "v_mfma_f32_16x16x32_bf16 %[c7], v[128:131], v[108:111], %[c7]\n" \
    "ds_read_b128 v[212:215], %[vb] offset:64\n" \
    "v_mfma_f32_16x16x32_bf16 %[c5], v[140:143], v[144:147], %[c5]\n" \
    "ds_read_b128 v[196:199], %[va] offset:576\n" \
    "v_mfma_f32_16x16x32_bf16 %[c6], v[140:143], v[132:135], %[c6]\n" \
    "ds_read_b128 v[208:211], %[vb] offset:1600\n" \
    "v_mfma_f32_16x16x32_bf16 %[c7], v[140:143], v[116:119], %[c7]\n" \
    "ds_read_b128 v[204:207], %[va] offset:1088\n" \
    "s_waitcnt lgkmcnt(6)\n" \
    "v_mfma_f32_16x16x32_bf16 %[c6], v[156:159], v[144:147], %[c6]\n" \
    "ds_read_b128 v[180:183], %[vb] offset:2112\n" \
    "v_mfma_f32_16x16x32_bf16 %[c7], v[156:159], v[132:135], %[c7]\n" \
    "ds_read_b128 v[200:203], %[va] offset:1600\n" \
    "v_mfma_f32_16x16x32_bf16 %[c7], v[152:155], v[144:147], %[c7]\n" \
    "ds_read_b128 v[176:179], %[vb] offset:2624\n" \
    "s_cmp_eq_u32 %[hf], 0\n" \
    "s_cbranch_scc1 LX2_%=\n" \
    "v_mfma_f32_16x16x32_bf16 %[c5], v[168:171], v[148:151], %[c5]\n" \
    "v_mfma_f32_16x16x32_bf16 %[c6], v[172:175], v[148:151], %[c6]\n" \
    "v_mfma_f32_16x16x32_bf16 %[c7], v[164:167], v[148:151], %[c7]\n" \
    "LX2_%=:\n" \
    "ds_read_b128 v[220:223], %[va] offset:2112\n" \
    "s_waitcnt lgkmcnt(4)\n" \
    "v_mfma_f32_16x16x32_bf16 %[c5], v[184:187], v[160:163], %[c5]\n" \
    "ds_read_b128 v[192:195], %[vb] offset:3136\n" \
    "v_mfma_f32_16x16x32_bf16 %[c6], v[184:187], v[188:191], %[c6]\n" \
    "ds_read_b128 v[96:99], %[va] offset:2624\n" \
    "v_mfma_f32_16x16x32_bf16 %[c7], v[184:187], v[212:215], %[c7]\n" \
    "ds_read_b128 v[112:115], %[vb] offset:3648\n" \
    "v_mfma_f32_16x16x32_bf16 %[c5], v[196:199], v[208:211], %[c5]\n" \
    "ds_read_b128 v[120:123], %[va] offset:3136\n" \
    "v_mfma_f32_16x16x32_bf16 %[c6], v[196:199], v[160:163], %[c6]\n" \
    "ds_read_b128 v[104:107], %[va] offset:3648\n" \
    "v_mfma_f32_16x16x32_bf16 %[c7], v[196:199], v[188:191], %[c7]\n" \
    "ds_read_b128 v[124:127], %[va] offset:128\n" \
    "s_waitcnt lgkmcnt(6)\n" \
    "v_mfma_f32_16x16x32_bf16 %[c5], v[204:207], v[180:183], %[c5]\n" \
    "ds_read_b128 v[216:219], %[vb] offset:1152\n" \
    "v_mfma_f32_16x16x32_bf16 %[c6], v[204:207], v[208:211], %[c6]\n" \
    "ds_read_b128 v[136:139], %[vb] offset:640\n" \
    "v_mfma_f32_16x16x32_bf16 %[c7], v[204:207], v[160:163], %[c7]\n" \
    "ds_read_b128 v[100:103], %[vb] offset:128\n" \
    "v_mfma_f32_16x16x32_bf16 %[c5], v[200:203], v[176:179], %[c5]\n" \
    "ds_read_b128 v[128:131], %[va] offset:640\n" \
    "v_mfma_f32_16x16x32_bf16 %[c6], v[200:203], v[180:183], %[c6]\n" \
    "ds_read_b128 v[108:111], %[vb] offset:1664\n" \
    "v_mfma_f32_16x16x32_bf16 %[c7], v[200:203], v[208:211], %[c7]\n" \
    "ds_read_b128 v[140:143], %[va] offset:1152\n" \
    "s_waitcnt lgkmcnt(6)\n" \
    "v_mfma_f32_16x16x32_bf16 %[c5], v[220:223], v[192:195], %[c5]\n" \
    "ds_read_b128 v[116:119], %[vb] offset:2176\n" \
    "v_mfma_f32_16x16x32_bf16 %[c6], v[220:223], v[176:179], %[c6]\n" \
    "ds_read_b128 v[156:159], %[va] offset:1664\n" \
    "v_mfma_f32_16x16x32_bf16 %[c7], v[220:223], v[180:183], %[c7]\n" \
    "ds_read_b128 v[132:135], %[vb] offset:2688\n" \
    "v_mfma_f32_16x16x32_bf16 %[c5], v[96:99], v[112:115], %[c5]\n" \
    "ds_read_b128 v[152:155], %[va] offset:2176\n" \
    "v_mfma_f32_16x16x32_bf16 %[c6], v[96:99], v[192:195], %[c6]\n" \
    "ds_read_b128 v[144:147], %[vb] offset:3200\n" \
    "v_mfma_f32_16x16x32_bf16 %[c7], v[96:99], v[176:179], %[c7]\n" \
    "ds_read_b128 v[168:171], %[va] offset:2688\n" \
    "v_mfma_f32_16x16x32_bf16 %[c6], v[120:123], v[112:115], %[c6]\n" \
    "ds_read_b128 v[172:175], %[vb] offset:3712\n" \
    "v_mfma_f32_16x16x32_bf16 %[c7], v[120:123], v[192:195], %[c7]\n" \
    "ds_read_b128 v[164:167], %[va] offset:3200\n" \
    "v_mfma_f32_16x16x32_bf16 %[c7], v[104:107], v[112:115], %[c7]\n" \
    "ds_read_b128 v[148:151], %[va] offset:3712\n" \
    "s_waitcnt lgkmcnt(6)\n" \
    "v_mfma_f32_16x16x32_bf16 %[c5], v[124:127], v[216:219], %[c5]\n" \
    "ds_read_b128 v[184:187], %[va] offset:192\n" \
    "v_mfma_f32_16x16x32_bf16 %[c6], v[124:127], v[136:139], %[c6]\n" \
    "ds_read_b128 v[212:215], %[vb] offset:1216\n" \
    "v_mfma_f32_16x16x32_bf16 %[c7], v[124:127], v[100:103], %[c7]\n" \
    "ds_read_b128 v[196:199], %[va] offset:704\n" \
    "v_mfma_f32_16x16x32_bf16 %[c5], v[128:131], v[108:111], %[c5]\n" \
    "ds_read_b128 v[188:191], %[vb] offset:1728\n" \
    "v_mfma_f32_16x16x32_bf16 %[c6], v[128:131], v[216:219], %[c6]\n" \
    "ds_read_b128 v[204:207], %[va] offset:1216\n" \
    "v_mfma_f32_16x16x32_bf16 %[c7], v[128:131], v[136:139], %[c7]\n" \
    "ds_read_b128 v[160:163], %[vb] offset:2240\n" \
    "v_mfma_f32_16x16x32_bf16 %[c5], v[140:143], v[116:119], %[c5]\n" \
    "ds_read_b128 v[200:203], %[va] offset:1728\n" \
    "v_mfma_f32_16x16x32_bf16 %[c6], v[140:143], v[108:111], %[c6]\n" \
    "ds_read_b128 v[208:211], %[vb] offset:2752\n" \
    "v_mfma_f32_16x16x32_bf16 %[c7], v[140:143], v[216:219], %[c7]\n" \
    "ds_read_b128 v[220:223], %[va] offset:2240\n" \
    "v_mfma_f32_16x16x32_bf16 %[c5], v[156:159], v[132:135], %[c5]\n" \
    "ds_read_b128 v[180:183], %[vb] offset:3264\n" \
    "v_mfma_f32_16x16x32_bf16 %[c6], v[156:159], v[116:119], %[c6]\n" \
    "ds_read_b128 v[96:99], %[va] offset:2752\n" \
    "v_mfma_f32_16x16x32_bf16 %[c7], v[156:159], v[108:111], %[c7]\n" \
    "ds_read_b128 v[176:179], %[vb] offset:3776\n" \
    "s_waitcnt lgkmcnt(6)\n" \
    "v_mfma_f32_16x16x32_bf16 %[c5], v[152:155], v[144:147], %[c5]\n" \
    "ds_read_b128 v[120:123], %[vb] offset:704\n" \
    "v_mfma_f32_16x16x32_bf16 %[c6], v[152:155], v[132:135], %[c6]\n" \
    "ds_read_b128 v[192:195], %[va] offset:3264\n" \
    "v_mfma_f32_16x16x32_bf16 %[c7], v[152:155], v[116:119], %[c7]\n" \
    "ds_read_b128 v[104:107], %[vb] offset:192\n" \
    "v_mfma_f32_16x16x32_bf16 %[c5], v[168:171], v[172:175], %[c5]\n" \
    "v_mfma_f32_16x16x32_bf16 %[c6], v[168:171], v[144:147], %[c6]\n" \
    "v_mfma_f32_16x16x32_bf16 %[c7], v[168:171], v[132:135], %[c7]\n" \
    "v_mfma_f32_16x16x32_bf16 %[c6], v[164:167], v[172:175], %[c6]\n" \
    "v_mfma_f32_16x16x32_bf16 %[c7], v[164:167], v[144:147], %[c7]\n" \
    "ds_read_b128 v[112:115], %[va] offset:3776\n" \
    "v_mfma_f32_16x16x32_bf16 %[c7], v[148:151], v[172:175], %[c7]\n" \
    "v_mfma_f32_16x16x32_bf16 %[c5], v[184:187], v[212:215], %[c5]\n" \
    "v_mfma_f32_16x16x32_bf16 %[c5], v[196:199], v[188:191], %[c5]\n" \
    "v_mfma_f32_16x16x32_bf16 %[c5], v[204:207], v[160:163], %[c5]\n" \
    "s_waitcnt lgkmcnt(1)\n" \
    "v_mfma_f32_16x16x32_bf16 %[c5], v[200:203], v[208:211], %[c5]\n" \
    "v_mfma_f32_16x16x32_bf16 %[c5], v[220:223], v[180:183], %[c5]\n" \
    "v_mfma_f32_16x16x32_bf16 %[c5], v[96:99], v[176:179], %[c5]\n" \
    "v_mfma_f32_16x16x32_bf16 %[c6], v[184:187], v[120:123], %[c6]\n" \
    "v_mfma_f32_16x16x32_bf16 %[c6], v[196:199], v[212:215], %[c6]\n" \
    "v_mfma_f32_16x16x32_bf16 %[c6], v[204:207], v[188:191], %[c6]\n" \
    "v_mfma_f32_16x16x32_bf16 %[c6], v[200:203], v[160:163], %[c6]\n" \
    "v_mfma_f32_16x16x32_bf16 %[c6], v[220:223], v[208:211], %[c6]\n" \
    "v_mfma_f32_16x16x32_bf16 %[c6], v[96:99], v[180:183], %[c6]\n" \
    "v_mfma_f32_16x16x32_bf16 %[c6], v[192:195], v[176:179], %[c6]\n" \
    "v_mfma_f32_16x16x32_bf16 %[c7], v[184:187], v[104:107], %[c7]\n" \
    "v_mfma_f32_16x16x32_bf16 %[c7], v[196:199], v[120:123], %[c7]\n" \
    "ds_write_b128 %[vy], %[c5] offset:5120\n" \
    "v_mfma_f32_16x16x32_bf16 %[c7], v[204:207], v[212:215], %[c7]\n" \
    "v_mfma_f32_16x16x32_bf16 %[c7], v[200:203], v[188:191], %[c7]\n" \
    "v_mfma_f32_16x16x32_bf16 %[c7], v[220:223], v[160:163], %[c7]\n" \
    "v_mfma_f32_16x16x32_bf16 %[c7], v[96:99], v[208:211], %[c7]\n" \
    "v_mfma_f32_16x16x32_bf16 %[c7], v[192:195], v[180:183], %[c7]\n" \
    "s_waitcnt lgkmcnt(1)\n" \
    "v_mfma_f32_16x16x32_bf16 %[c7], v[112:115], v[176:179], %[c7]\n" \
    "ds_write_b128 %[vy], %[c6] offset:6144\n" \
    "s_nop 6\n" \
    "ds_write_b128 %[vy], %[c7] offset:7168\n" \
    "s_waitcnt lgkmcnt(0)\n" \
    "s_barrier\n"
#define FRAG_REGS \
    "v96", "v97", "v98", "v99", "v100", "v101", "v102", "v103", "v104", "v105", "v106", "v107", "v108", "v109", "v110", "v111", \
    "v112", "v113", "v114", "v115", "v116", "v117", "v118", "v119", "v120", "v121", "v122", "v123", "v124", "v125", "v126", "v127", \
    "v128", "v129", "v130", "v131", "v132", "v133", "v134", "v135", "v136", "v137", "v138", "v139", "v140", "v141", "v142", "v143", \
    "v144", "v145", "v146", "v147", "v148", "v149", "v150", "v151", "v152", "v153", "v154", "v155", "v156", "v157", "v158", "v159", \
    "v160", "v161", "v162", "v163", "v164", "v165", "v166", "v167", "v168", "v169", "v170", "v171", "v172", "v173", "v174", "v175", \
    "v176", "v177", "v178", "v179", "v180", "v181", "v182", "v183", "v184", "v185", "v186", "v187", "v188", "v189", "v190", "v191", \
    "v192", "v193", "v194", "v195", "v196", "v197", "v198", "v199", "v200", "v201", "v202", "v203", "v204", "v205", "v206", "v207", \
    "v208", "v209", "v210", "v211", "v212", "v213", "v214", "v215", "v216", "v217", "v218", "v219", "v220", "v221", "v222", "v223"

__device__ __forceinline__ float wave_sum63(float v) {
  int x;
#define DPP_ADD(ctrl, rmask)                                                               \
  x = __builtin_amdgcn_update_dpp(0, __builtin_bit_cast(int, v), ctrl, rmask, 0xf, false); \
  v += __builtin_bit_cast(float, x);
  DPP_ADD(0x111, 0xf)
  DPP_ADD(0x112, 0xf)
  DPP_ADD(0x114, 0xf)
  DPP_ADD(0x118, 0xf)
  DPP_ADD(0x142, 0xa)
  DPP_ADD(0x143, 0xc)
#undef DPP_ADD
  return v;
}
__device__ __forceinline__ u32 lds_arrive(int word, int lane) {
  asm volatile("" ::: "memory");
  u32 old = 0;
  if (lane == 0) old = __hip_atomic_fetch_add((LAS u32*)(uintptr_t)(F_OFF + 4 * word), 1u, RLX_WG);
  old = __builtin_amdgcn_readfirstlane(old);
  asm volatile("" ::: "memory");
  return old;
}
__device__ __forceinline__ void lds_wait(int word, u32 target) {
#pragma nounroll
  for (unsigned spins = 0; spins < SPIN_CAP; ++spins) {
    if (__builtin_amdgcn_readfirstlane(__hip_atomic_load((LAS u32*)(uintptr_t)(F_OFF + 4 * word), RLX_WG)) >= target) break;
  }
  asm volatile("" ::: "memory");
}
__device__ __forceinline__ void poll_flags(gu64* fl, u64 tag) {
#pragma nounroll
  for (unsigned spins = 0; spins < SPIN_CAP; ++spins) {
    if (__all(__hip_atomic_load(fl, RLX_AGENT) == tag)) break;
    __builtin_amdgcn_s_sleep(4);
  }
  __builtin_amdgcn_fence(__ATOMIC_ACQUIRE, "wavefront");
}

__global__ __launch_bounds__(512, 2) void fused(
    const u16* __restrict__ u, const u16* __restrict__ Are, const u16* __restrict__ Aim, const u16* __restrict__ Bre,
    const u16* __restrict__ Bim, const u16* __restrict__ Cre, const u16* __restrict__ Cim,
    const u16* __restrict__ logdt, const u16* __restrict__ Dskip, const u16* __restrict__ kk,
    const u16* __restrict__ vv, const u16* __restrict__ sk, const u16* __restrict__ ssmk,
    const u16* __restrict__ ssmD, const u16* __restrict__ gw, u16* __restrict__ out, char* __restrict__ ws) {
  extern __shared__ __attribute__((aligned(16))) char smem[];
  const int tid = threadIdx.x;
  const int lane = tid & 63;
  const int w = __builtin_amdgcn_readfirstlane(tid >> 6);
  const int b = blockIdx.x;
  const int xg = b & 7, tt = b >> 3;
  const int jc = xg * 32 + tt;
  const int d0 = 2 * jc;
  asm volatile("" ::"s"(u), "s"(Are), "s"(Aim), "s"(Bre), "s"(Bim), "s"(Cre), "s"(Cim), "s"(logdt), "s"(Dskip));
  asm volatile("" ::"s"(kk), "s"(vv), "s"(sk), "s"(ssmk), "s"(ssmD), "s"(gw), "s"(out), "s"(ws));
  const __amdgpu_buffer_rsrc_t xrs = __builtin_amdgcn_make_buffer_rsrc(ws + WS_X, 0, 256 * XS, 0x00020000);
  const __amdgpu_buffer_rsrc_t grs = __builtin_amdgcn_make_buffer_rsrc(ws + WS_GRAN, 0, 2048 * 8, 0x00020000);
  const __amdgpu_buffer_rsrc_t frs = __builtin_amdgcn_make_buffer_rsrc(ws + WS_FLAG, 0, 512 * 128, 0x00020000);
  const __amdgpu_buffer_rsrc_t rsu = __builtin_amdgcn_make_buffer_rsrc((void*)u, 0, L * D * 2, 0x00020000);
  const u32x4 z4 = {0u, 0u, 0u, 0u};
  const int w4 = w & 3;
  const int col = xg * 32 + (w & 1) * 16 + (lane & 15);
  const int t0 = 64 * tt + 32 * (w4 >> 1) + 8 * (lane >> 4);
  const int vo = t0 * 1024 + col * 4;
  const int io = 255 - (t0 >> 3);
  const int xw = (col >> 1) * (2 * XS) + (io >> 2) * 128 + (col & 1) * 64 + (io & 3) * 16;
  const int xr = (jc >> 1) * (2 * XS) + (jc & 1) * 64;
  const int m = lane & 15, h = lane >> 4;
  const int gch = w4 >> 1, hw = w & 1;
  const int pi = (d0 + gch) * N + hw * 64 + lane;
  u32 are = Are[pi], aim = Aim[pi], ldt = logdt[d0 + gch];
  char* const et = smem + KA_OFF + w4 * ET;
  char* const rowp = et + lane * 4;
  auto put = [&](int e, f32x2 a) { *(u32*)(rowp + e * ES) = pkv2(a); };
  u64 tag;
  if (w < 4) {
    const __amdgpu_buffer_rsrc_t rsk = __builtin_amdgcn_make_buffer_rsrc((void*)kk, 0, L * D * 2, 0x00020000);
    const __amdgpu_buffer_rsrc_t rsv = __builtin_amdgcn_make_buffer_rsrc((void*)vv, 0, L * D * 2, 0x00020000);
    const u16 bre = Bre[pi], bim = Bim[pi], cre = Cre[pi], cim = Cim[pi];
    asm volatile("" ::: "memory");
    if (tid < 128) *(u32x4*)(smem + UR_OFF + (tid >> 5) * URB + 16 * (258 + (tid & 31))) = z4;
    else if (tid < 136) *(u32x4*)(smem + UR_OFF + ((tid >> 1) & 3) * URB + 16 * (tid & 1)) = z4;
    else if (tid < 144) *(u32x4*)(smem + F_OFF + 16 * (tid - 136)) = z4;
    __builtin_amdgcn_sched_barrier(0);
    u32x4 skv = *(const u32x4*)(sk + col * 8);
    u32x4 gwv = *(const u32x4*)(gw + lane * 8);
    tag = __hip_atomic_load((gu64*)ws, RLX_AGENT);
    u32 rk[11], rv[8];
#pragma unroll
    for (int i = 0; i < 3; ++i) rk[i] = __builtin_amdgcn_raw_buffer_load_b32(rsk, vo - 3072 + i * 1024, 0, 0);
#pragma unroll
    for (int i = 0; i < 8; ++i) {
      rk[3 + i] = __builtin_amdgcn_raw_buffer_load_b32(rsk, vo + (i & 3) * 1024, (i >> 2) * 4096, 0);
      rv[i] = __builtin_amdgcn_raw_buffer_load_b32(rsv, vo + (i & 3) * 1024, (i >> 2) * 4096, 0);
    }
    __builtin_amdgcn_sched_barrier(0);
    asm volatile("" : "+v"(are), "+v"(aim), "+v"(ldt)::"memory");
    {
      const float dt = rbf(expf(bflo(ldt)));
      const float ar = (dt * bflo(are)) * 1.4426950408889634f;
      const float ai = (dt * bflo(aim)) * 0.15915494309189535f;
      const f32x2 e128 = cexp_l(ar, ai, 128.f), f8 = cexp_l(ar, -ai, 8.f);
      const f32x2 e256 = cmul(e128, e128), e512 = cmul(e256, e256), e1024 = cmul(e512, e512);
      const f32x2 e384 = cmul(e256, e128);
      const f32x2 c[2] = {e1024, cmul(e1024, e512)};
#pragma unroll
      for (int i = 0; i < 2; ++i) {
        put(32 + 4 * i, c[i]);
        put(33 + 4 * i, cmul(c[i], e128));
        put(34 + 4 * i, cmul(c[i], e256));
        put(35 + 4 * i, cmul(c[i], e384));
      }
      put(40, cmul(c[1], e512));
      const f32x2 f16 = cmul(f8, f8), f32 = cmul(f16, f16), f64 = cmul(f32, f32);
      const f32x2 f24 = cmul(f16, f8);
      const f32x2 Bc = {dt * bf2f(bre), -(dt * bf2f(bim))}, Cc = {bf2f(cre), -bf2f(cim)};
      f32x2 z[4];
      z[0] = cmul(Cc, Bc);
      z[1] = cmul(z[0], f32);
      z[2] = cmul(z[0], f64);
      z[3] = cmul(z[2], f32);
#pragma unroll
      for (int i = 0; i < 4; ++i) {
        put(41 + 4 * i, z[i]);
        put(42 + 4 * i, cmul(z[i], f8));
        put(43 + 4 * i, cmul(z[i], f16));
        put(44 + 4 * i, cmul(z[i], f24));
      }
    }
    asm volatile("" : "+v"(tag)::"memory");
    __builtin_amdgcn_sched_barrier(0);
    __syncthreads();
    __builtin_amdgcn_s_setprio(1);
    __builtin_amdgcn_sched_barrier(0);
    asm volatile("" : "+v"(skv), "+v"(gwv), "+v"(tag)::"memory");
    tag += 1ull;
    const int grow = 8 * b + 4 * (w & 1);
    u32x4 gr[4];
    gu64* fl = (gu64*)(ws + WS_FLAG + (xg * 32 + 24 - 8 * (w & 1) - 2 * (lane & 8) + (lane & 7)) * 128);
    u64 f = 0;
    {
      const float s0[4] = {bflo(skv[0]), bfhi(skv[0]), bflo(skv[1]), bfhi(skv[1])};
      const float s1[4] = {bflo(skv[2]), bfhi(skv[2]), bflo(skv[3]), bfhi(skv[3])};
      asm volatile("" : "+v"(rv[7]), "+v"(rk[10])::"memory");
      {
        const int go = (w >= 2) ? grow * 1024 + lane * 16 : 0x7fff0000;
#pragma unroll
        for (int i = 0; i < 4; ++i) gr[i] = __builtin_amdgcn_raw_buffer_load_b128(rsu, go + i * 1024, 0, 0);
      }
      asm volatile("" ::: "memory");
      __builtin_amdgcn_sched_barrier(0);
      f32x2 kf[11];
      u32 kv[8];
#pragma unroll
      for (int i = 0; i < 11; ++i) kf[i] = (f32x2){bflo(rk[i]), bfhi(rk[i])};
#pragma unroll
      for (int i = 0; i < 8; ++i) {
        u32 acc = pkv2(kf[i] * (f32x2){s0[3], s1[3]});
#pragma unroll
        for (int j = 1; j < 4; ++j) {
          const u32 p = pkv2(kf[i + j] * (f32x2){s0[3 - j], s1[3 - j]});
          acc = pkv2((f32x2){bflo(acc), bfhi(acc)} + (f32x2){bflo(p), bfhi(p)});
        }
        kv[i] = pkv2((f32x2){bflo(acc), bfhi(acc)} * (f32x2){bflo(rv[i]), bfhi(rv[i])});
        if (i == 6) {
#pragma unroll
          for (int k = 0; k <= 6; ++k) asm volatile("" : "+v"(kv[k])::"memory");
          __builtin_amdgcn_sched_barrier(0);
          const u32x2 f2 = __builtin_amdgcn_raw_buffer_load_b64(
              frs, (w < 2) ? (xg * 32 + 24 - 8 * (w & 1) - 2 * (lane & 8) + (lane & 7)) * 128 : 0x7fff0000, 0, 16);
          f = (u64)f2[0] | ((u64)f2[1] << 32);
          asm volatile("" ::: "memory");
          __builtin_amdgcn_sched_barrier(0);
        }
      }
      u32x4 p1, p3;
#pragma unroll
      for (int j = 0; j < 4; ++j) {
        p1[j] = __builtin_amdgcn_perm(kv[6 - 2 * j], kv[7 - 2 * j], 0x05040100u);
        p3[j] = __builtin_amdgcn_perm(kv[6 - 2 * j], kv[7 - 2 * j], 0x07060302u);
      }
      __builtin_amdgcn_raw_buffer_store_b128(p1, xrs, xw, 8192, 16);
      __builtin_amdgcn_raw_buffer_store_b128(p3, xrs, xw, 3 * 8192, 16);
    }
    __builtin_amdgcn_sched_barrier(0);
    asm volatile("s_nop 1" ::: "memory");
    __builtin_amdgcn_sched_barrier(0);
    auto publish_kv = [&]() {
      asm volatile("s_waitcnt vmcnt(0)" ::: "memory");
      const u32 old = lds_arrive(5, lane);
      const u32x4 fv = {(u32)tag, (u32)(tag >> 32), (u32)tag, (u32)(tag >> 32)};
      if (old == 3 && lane < 8) __builtin_amdgcn_raw_buffer_store_b128(fv, frs, (256 + jc) * 128 + lane * 16, 0, 16);
      asm volatile("" ::: "memory");
    };
    if (w >= 2) {
      float s = 0.f;
#pragma unroll
      for (int i = 0; i < 4; ++i) {
        float a = dot2bf(gr[i][0], gwv[0], 0.f);
#pragma unroll
        for (int j = 1; j < 4; ++j) a = dot2bf(gr[i][j], gwv[j], a);
        const float tot = __builtin_bit_cast(float, __builtin_amdgcn_readlane(__builtin_bit_cast(int, wave_sum63(a)), 63));
        s = (lane == i) ? tot : s;
      }
      const u32 gb = pk2(__builtin_amdgcn_rcpf(1.f + __expf(-rbf(s))), 0.f) & 0xffffu;
      LAS u32* gs = (LAS u32*)(uintptr_t)(F_OFF + 32);
      if (lane < 4) gs[4 * (w - 2) + lane] = gb;
      const bool lastg = lds_arrive(7, lane) == 1;
      publish_kv();
      if (lastg && lane < 4) {
        const u32x2 g2 = *(LAS u32x2*)(gs + 2 * lane);
        const u32 tl = (u32)(tag << 16), th = (u32)(tag >> 16);
        __builtin_amdgcn_raw_buffer_store_b128((u32x4){tl | g2[0], th, tl | g2[1], th}, grs, b * 64 + lane * 16, 0, 16);
      }
    } else {
      const bool early = __all(f == tag);
      if (!early) {
        publish_kv();
        poll_flags(fl, tag);
      }
      __builtin_amdgcn_fence(__ATOMIC_ACQUIRE, "wavefront");
      const int xo = xr + (tid >> 2) * 128 + (tid & 3) * 16;
      u32x4 x[4];
#pragma unroll
      for (int c = 0; c < 4; ++c) x[c] = __builtin_amdgcn_raw_buffer_load_b128(xrs, xo, (c >> 1) * 16384 + (c & 1) * 4096, 16);
      char* dst = smem + UR_OFF + 32 + 16 * tid;
#pragma unroll
      for (int c = 0; c < 4; ++c) *(u32x4*)(dst + (c >> 1) * 2 * URB + (c & 1) * 2048) = x[c];
      if (early) publish_kv();
    }
    const int ch = w & 1, hf = w >> 1;
    const int la = ch * 4 * CS + (m & 3) * CS + 2 * ((m & ~3) + 8 * h) + 256 * hf;
    const int lb = UR_OFF + 2 * ch * URB + 480 - 32 * m + 16 * h + 256 * hf;
    const int ly = (2 * ch + hf) * 8192 + 64 * m + 16 * ((h + (m >> 1)) & 3);
    f32x4 c0, c1, c2, c3, c4, c5, c6, c7;
    int va = KA_OFF + la, vb = lb, vy = Y_OFF + ly;
    int va2 = KB_OFF + (ch * 8 + (m & 7)) * CS + 2 * ((m & 8) + 8 * h) + 256 * hf, vb2 = lb + URB, vy2 = KA_OFF + ly;
    asm volatile("" : "+v"(va), "+v"(vb), "+v"(vy), "+v"(vy2), "+v"(va2), "+v"(vb2));
    __builtin_amdgcn_s_setprio(0);
    __syncthreads();
    asm volatile(
        CONV_A_ASM
        : [c0] "=&v"(c0), [c1] "=&v"(c1), [c2] "=&v"(c2), [c3] "=&v"(c3), [c4] "=&v"(c4), [c5] "=&v"(c5), [c6] "=&v"(c6),
          [c7] "=&v"(c7)
        : [va] "v"(va), [vb] "v"(vb), [vy] "v"(vy), [hf] "s"(hf)
        : "memory", "scc", FRAG_REGS);
    __builtin_amdgcn_s_barrier();
    asm volatile(
        CONV_B_ASM
        : [c0] "=&v"(c0), [c1] "=&v"(c1), [c2] "=&v"(c2), [c3] "=&v"(c3), [c4] "=&v"(c4), [c5] "=&v"(c5), [c6] "=&v"(c6),
          [c7] "=&v"(c7)
        : [va] "v"(va2), [vb] "v"(vb2), [vy] "v"(vy2), [hf] "s"(hf)
        : "memory", "scc", FRAG_REGS);
  } else {
    __builtin_amdgcn_s_setprio(3);
    const __amdgpu_buffer_rsrc_t rss = __builtin_amdgcn_make_buffer_rsrc((void*)ssmk, 0, L * D * 2, 0x00020000);
    const int ch = gch;
    u32 ru[8];
#pragma unroll
    for (int i = 0; i < 8; ++i) ru[i] = __builtin_amdgcn_raw_buffer_load_b32(rsu, vo + (i & 3) * 1024, (i >> 2) * 4096, 0);
    const u16 dsv = Dskip[d0 + ch], sdv = ssmD[d0 + ch];
    tag = __hip_atomic_load((gu64*)ws, RLX_AGENT);
    __builtin_amdgcn_s_setprio(2);
    __builtin_amdgcn_sched_barrier(0);
    const int sc = hw * 64 + lane;
    const u32 cb = (u32)(CT_OFF + ch * CTB) - 8u * 16255u;
    u32 sco[3][4];
#pragma unroll
    for (int tl = 0; tl < 3; ++tl) {
      const u64 lt2 = (tl == 0) ? 0x0003000200010000ull : (tl == 1) ? 0x0300020001020100ull : 0x1000080006000400ull;
      const float lf = (float)((int)((lt2 >> (16 * h)) & 0xffffull) + 8 * m);
      const float d1 = (tl == 2) ? 128.f : 4.f, d2 = (tl == 2) ? 256.f : 128.f;
#pragma unroll
      for (int q = 0; q < 4; ++q) {
        const float l = lf + ((q & 1) ? d1 : 0.f) + ((q & 2) ? d2 : 0.f);
        u32 ci = __float_as_uint(l);
        if (tl == 2) ci = (l <= 2048.f) ? ci : 0x45010000u;
        ci >>= 16;
        if (tl == 0 && q == 0) ci = max(ci, 16255u);
        sco[tl][q] = (ci << 3) + cb + 4u * hw;
      }
    }
#pragma unroll
    for (int q = 0; q < 4; ++q) asm volatile("" : "+v"(sco[0][q]), "+v"(sco[1][q]), "+v"(sco[2][q]));
    asm volatile("" : "+v"(are), "+v"(aim), "+v"(ldt)::"memory");
    {
      const float dt = rbf(expf(bflo(ldt)));
      const float ar = (dt * bflo(are)) * 1.4426950408889634f;
      const float ai = (dt * bflo(aim)) * 0.15915494309189535f;
      f32x2 e[4];
      e[1] = cexp_l(ar, ai, 1.f);
      const f32x2 e128 = cexp_l(ar, ai, 128.f);
      e[2] = cmul(e[1], e[1]);
      e[3] = cmul(e[2], e[1]);
      const f32x2 e4 = cmul(e[2], e[2]);
      const f32x2 e132 = cmul(e128, e4);
      *(u32*)(rowp) = 0x00003f80u;
      put(1, e4); put(2, e128); put(3, e132);
      {
        __builtin_amdgcn_sched_barrier(0);
        u32x4 p0, p2;
#pragma unroll
        for (int j = 0; j < 4; ++j) {
          p0[j] = __builtin_amdgcn_perm(ru[6 - 2 * j], ru[7 - 2 * j], 0x05040100u);
          p2[j] = __builtin_amdgcn_perm(ru[6 - 2 * j], ru[7 - 2 * j], 0x07060302u);
        }
        __builtin_amdgcn_raw_buffer_store_b128(p0, xrs, xw, 0, 16);
        __builtin_amdgcn_raw_buffer_store_b128(p2, xrs, xw, 2 * 8192, 16);
        __builtin_amdgcn_sched_barrier(0);
        asm volatile("s_nop 1" ::: "memory");
        __builtin_amdgcn_sched_barrier(0);
      }
#pragma unroll
      for (int i = 1; i < 4; ++i) {
        put(4 * i, e[i]);
        put(4 * i + 1, cmul(e[i], e4));
        put(4 * i + 2, cmul(e[i], e128));
        put(4 * i + 3, cmul(e[i], e132));
      }
      const f32x2 e256 = cmul(e128, e128), e512 = cmul(e256, e256);
      const f32x2 a[4] = {e256, cmul(e256, e[2]), e512, cmul(e512, e256)};
#pragma unroll
      for (int i = 0; i < 4; ++i) {
        put(16 + 4 * i, a[i]);
        put(17 + 4 * i, cmul(a[i], e4));
        put(18 + 4 * i, cmul(a[i], e128));
        put(19 + 4 * i, cmul(a[i], e132));
      }
    }
    asm volatile("" : "+v"(tag)::"memory");
    tag += 1ull;
    __builtin_amdgcn_sched_barrier(0);
    __syncthreads();
    asm volatile("s_waitcnt vmcnt(0)" ::: "memory");
    {
      const u32 old = lds_arrive(3, lane);
      const u32x4 fv = {(u32)tag, (u32)(tag >> 32), (u32)tag, (u32)(tag >> 32)};
      if (old == 3 && lane < 8) __builtin_amdgcn_raw_buffer_store_b128(fv, frs, jc * 128 + lane * 16, 0, 16);
    }
    {
      const char* pa = et + m * ES + h * 16;
      bf16x8 fa[3][4], fb[4];
#pragma unroll
      for (int ks = 0; ks < 4; ++ks) {
        fb[ks] = *(const bf16x8*)(pa + 41 * ES + ks * 64);
#pragma unroll
        for (int tl = 0; tl < 3; ++tl) fa[tl][ks] = *(const bf16x8*)(pa + tl * 16 * ES + ks * 64);
      }
      __builtin_amdgcn_sched_barrier(0);
      f32x4 acc[3];
#pragma unroll
      for (int tl = 0; tl < 3; ++tl) acc[tl] = (f32x4){0.f, 0.f, 0.f, 0.f};
      if (sc == 0) acc[0][0] = bf2f(dsv);
#pragma unroll
      for (int ks = 0; ks < 4; ++ks)
#pragma unroll
        for (int tl = 0; tl < 3; ++tl)
          acc[tl] = __builtin_amdgcn_mfma_f32_16x16x32_bf16(fa[tl][ks], fb[ks], acc[tl], 0, 0, 0);
#pragma unroll
      for (int tl = 0; tl < 3; ++tl)
#pragma unroll
        for (int q = 0; q < 4; ++q) *(LAS float*)(uintptr_t)sco[tl][q] = acc[tl][q];
    }
    lds_arrive(6, lane);
    u32x4 kl[2], kn[2];
#pragma unroll
    for (int r = 0; r < 2; ++r) {
      kl[r] = __builtin_amdgcn_raw_buffer_load_b128(rss, (d0 + ch) * L * 2 + sc * 16 + r * 2048, 0, 0);
      kn[r] = __builtin_amdgcn_raw_buffer_load_b128(rss, (d0 + ch) * L * 2 + sc * 16 + r * 2048 + 16, 0, 0);
    }
    u32 exo[20];
    {
      const float lf = (float)(16 * sc);
#pragma unroll
      for (int j = 0; j < 10; ++j) {
        const u32 bits = pk2(lf + (float)(2 * j), lf + (float)(2 * j + 1));
        u32 lo = bits & 0xffffu;
        if (j == 0) lo = max(lo, 16255u);
        exo[2 * j] = (lo << 3) + cb;
        exo[2 * j + 1] = ((bits >> 16) << 3) + cb;
      }
    }
#pragma unroll
    for (int j = 0; j < 20; ++j) asm volatile("" : "+v"(exo[j]));
    lds_wait(6, 4);
    {
      float val[20];
#pragma unroll
      for (int j = 0; j < 20; ++j) {
        const f32x2 p = *(const LAS f32x2*)(uintptr_t)exo[j];
        val[j] = sadd(p.x, p.y);
      }
      lds_arrive(18, lane);
      if (sc == 127) { val[16] = 0.f; val[17] = 0.f; val[18] = 0.f; val[19] = 0.f; }
      char* kp = smem + KA_OFF + ch * 4 * CS;
#pragma unroll
      for (int e = 0; e < 4; ++e) {
#pragma unroll
        for (int g = 0; g < 2; ++g) {
          u32x4 o;
#pragma unroll
          for (int k = 0; k < 4; ++k)
            o[k] = cvt_once(val[e + 1 + 8 * g + 2 * k], val[e + 2 + 8 * g + 2 * k]);
          *(u32x4*)(kp + e * CS + 64 + 32 * sc + 16 * g) = o;
        }
      }
      if (hw == 0) {
        const u32 v01 = __builtin_amdgcn_readfirstlane(pk2(val[0], val[1])),
                  v23 = __builtin_amdgcn_readfirstlane(pk2(val[2], val[3]));
        const u64 k03 = (u64)v01 | ((u64)v23 << 32);
        const int e = (lane >> 3) & 3, k = lane & 7;
        const u64 tail = (k == 3) ? (k03 << (48 - 16 * e)) : 0ull;
        if (lane < 32 && k < 6)
          *(u32x4*)(kp + e * CS + ((k < 4) ? 16 * k : 64 + 16 * (252 + k))) = (u32x4){0u, 0u, (u32)tail, (u32)(tail >> 32)};
      }
    }
    asm volatile("" : "+v"(kl[0]), "+v"(kl[1]), "+v"(kn[0]), "+v"(kn[1])::"memory");
    lds_wait(18, 4);
    {
      char* kp = smem + KB_OFF + ch * 8 * CS;
      if (sc == 127) kn[1] = z4;
      if (hw == 0) {
        const int e = lane >> 3, i = lane & 7;
        if (i < 5) *(u32x4*)(kp + e * CS + ((i < 4) ? 16 * i : 64 + 4096)) = z4;
        const u32 k01 = __builtin_amdgcn_readfirstlane((pk2(sadd(bflo(kl[0][0]), bf2f(sdv)), 0.f) & 0xffffu) |
                                                       (kl[0][0] & 0xffff0000u)),
                  k23 = __builtin_amdgcn_readfirstlane(kl[0][1]), k45 = __builtin_amdgcn_readfirstlane(kl[0][2]),
                  k67 = __builtin_amdgcn_readfirstlane(kl[0][3]);
        const u32 v = (i < 4) ? ((i < 2) ? k01 : k23) : ((i < 6) ? k45 : k67);
        if (i <= e) *(u16*)(kp + e * CS + 2 * (31 - e + i)) = (u16)(v >> (16 * (i & 1)));
      }
#pragma unroll
      for (int r = 0; r < 2; ++r) {
        char* p = kp + 64 + 16 * sc + 2048 * r;
        const u32 d[8] = {kl[r][0], kl[r][1], kl[r][2], kl[r][3], kn[r][0], kn[r][1], kn[r][2], kn[r][3]};
#pragma unroll
        for (int e = 0; e < 7; ++e) {
          u32x4 o;
#pragma unroll
          for (int k = 0; k < 4; ++k) {
            const int i = k + ((e + 1) >> 1);
            o[k] = (e & 1) ? shf_once(d[i], d[i], 0) : shf_once(d[i + 1], d[i], 16);
          }
          *(u32x4*)(p + e * CS) = o;
          if (r == 0 && e == 6) __syncthreads();
        }
        *(u32x4*)(p + 7 * CS) = kn[r];
      }
    }
    poll_flags((gu64*)(ws + WS_FLAG + (256 + xg * 32 + 24 - 8 * w4 + (lane & 7)) * 128), tag);
    {
      const int i2 = tid - 256;
      const int xo = xr + (i2 >> 2) * 128 + (i2 & 3) * 16;
      const u32x4 x0 = __builtin_amdgcn_raw_buffer_load_b128(xrs, xo, 8192, 16);
      const u32x4 x1 = __builtin_amdgcn_raw_buffer_load_b128(xrs, xo, 3 * 8192, 16);
      char* dst = smem + UR_OFF + 32 + 16 * i2;
      *(u32x4*)(dst + URB) = x0;
      *(u32x4*)(dst + 3 * URB) = x1;
    }
    const int t = 64 * w4 + lane;
    const int yo = ((t >> 4) << 6) + 16 * (((t >> 2) + (t >> 5)) & 3) + 4 * (t & 3);
    u32x2 ga[8];
#pragma nounroll
    for (unsigned spins = 0; spins < SPIN_CAP; ++spins) {
      asm volatile("" ::: "memory");
#pragma unroll
      for (int k = 0; k < 8; ++k) ga[k] = __builtin_amdgcn_raw_buffer_load_b64(grs, t * 8 + (k & 1) * 2048, (k >> 1) * 4096, 16);
      const u32 tl = (u32)(tag << 16), th = (u32)(tag >> 16);
      u32 bad = 0, bad2 = 0;
#pragma unroll
      for (int k = 0; k < 8; ++k) {
        bad |= ga[k][0] ^ tl;
        bad2 |= ga[k][1] ^ th;
      }
      if (__all(((bad >> 16) | bad2) == 0)) break;
      __builtin_amdgcn_s_sleep(4);
    }
    __builtin_amdgcn_s_setprio(3);
    __syncthreads();
    GAS u32* op = (GAS u32*)(out + t * D + d0);
    const char* px = smem + UR_OFF + 2 * (16 + 2047 - t);
    const char* pya = smem + Y_OFF + yo;
    const char* pyb = smem + KA_OFF + yo;
    float cf[8][2], pa[8][2];
    auto pre = [&](int k) {
      const float g = bflo(ga[k][0]);
#pragma unroll
      for (int c = 0; c < 2; ++c) {
        const float xv = bf2f(*(const u16*)(px + 2 * c * URB - 512 * k));
        const float y0 = *(const float*)(pya + (2 * c) * 8192 + 1024 * k);
        const float y1 = *(const float*)(pya + (2 * c + 1) * 8192 + 1024 * k);
        cf[k][c] = sfnma(g, xv, xv);
        pa[k][c] = smul(g, sadd(y0, y1));
        asm volatile("" : "+v"(cf[k][c]), "+v"(pa[k][c]));
      }
    };
    auto post = [&](int k) {
      float r[2];
#pragma unroll
      for (int c = 0; c < 2; ++c) {
        const float y0 = *(const float*)(pyb + (2 * c) * 8192 + 1024 * k);
        const float y1 = *(const float*)(pyb + (2 * c + 1) * 8192 + 1024 * k);
        r[c] = sfma(cf[k][c], sadd(y0, y1), pa[k][c]);
      }
      op[k * 256 * (D / 2)] = pk2(r[0], r[1]);
    };
    constexpr int NPH = 3;
    constexpr int PH[NPH + 1] = {0, 2, 5, 8};
#pragma unroll
    for (int p = 0; p < NPH; ++p) {
#pragma unroll
      for (int k = PH[p]; k < PH[p + 1]; ++k) pre(k);
      if (p == 1 && b == 0) {
        if (lds_arrive(4, lane) == 3 && lane == 0) __hip_atomic_store((gu64*)ws, tag, RLX_AGENT);
      }
      asm volatile("s_waitcnt lgkmcnt(0)" ::: "memory");
      __builtin_amdgcn_s_barrier();
      asm volatile("" ::: "memory");
#pragma unroll
      for (int k = PH[p]; k < PH[p + 1]; ++k) post(k);
      asm volatile("" ::: "memory");
    }
  }
}

extern "C" void kernel_launch(void* const* d_in, const int* in_sizes, int n_in, void* d_out, int out_size, void* d_ws,
                              size_t ws_size, hipStream_t stream) {
  const u16* const* in = (const u16* const*)d_in;
  (void)hipFuncSetAttribute((const void*)fused, hipFuncAttributeMaxDynamicSharedMemorySize, LDS_BYTES);
  fused<<<256, 512, LDS_BYTES, stream>>>(in[0], in[1], in[2], in[3], in[4], in[5], in[6], in[7], in[8], in[10], in[11],
                                         in[12], in[13], in[14], in[15], (u16*)d_out, (char*)d_ws);
}
```
